```python
import torch

def _softmax(x, axis=-1):
    return torch.softmax(x, dim=axis)
def _gelu(x):
    return torch.nn.functional.gelu(x, approximate="tanh")
def _silu(x):
    return torch.nn.functional.silu(x)
def _sigmoid(x):
    return torch.sigmoid(x)
def _layernorm(x, axis=-1, eps=1e-5):
    m = x.mean(dim=axis, keepdim=True)
    v = x.var(dim=axis, keepdim=True, unbiased=False)
    return (x - m) / torch.sqrt(v + eps)
def _scatter_add(tgt, idx, src, axis=0):
    return tgt.clone().index_add_(axis, idx, src)
def _segment_sum(src, seg, n):
    out = torch.zeros((n,) + src.shape[1:], dtype=src.dtype)
    return out.index_add_(0, seg, src)
def _pad(x, pw):
    flat = tuple(v for pair in reversed(pw) for v in pair)
    return torch.nn.functional.pad(x, flat)

kernel_name = 'standalone__sliding_window_attn__prompt'

def setup_inputs():

    _w = (lambda a: (lambda t: t.to(torch.bfloat16) if t.is_floating_point() else t)(torch.as_tensor(a)))
    B, H, S, D = 1, 32, 2048, 192
    return {
        "q": _w((lambda *s: torch.randn(*s, dtype=torch.bfloat16) * 0.8)(B, H, S, D)),
        "k": _w((lambda *s: torch.randn(*s, dtype=torch.bfloat16) * 0.8)(B, H, S, D)),
        "v": _w((lambda *s: torch.randn(*s, dtype=torch.bfloat16) * 0.05)(B, H, S, D)),
        "window": 256,
    }

def reference(q, k, v, window):
    B, H, S, D = q.shape
    scale = D ** -0.5
    scores = torch.einsum("bhqd,bhkd->bhqk", q, k) * scale
    idx = torch.arange(S)
    mask = (idx[:, None] - idx[None, :] >= 0) & (idx[:, None] - idx[None, :] < window)
    scores = torch.where(mask[None, None], scores, -1e9)
    p = _softmax(scores, axis=-1)
    return torch.einsum("bhqk,bhkd->bhqd", p, v)
```

```cpp
#include <hip/hip_runtime.h>
#include <hip/hip_bf16.h>
#include <stdint.h>

typedef short bf16x8 __attribute__((ext_vector_type(8)));
typedef short s16x4 __attribute__((ext_vector_type(4)));
typedef float f32x16 __attribute__((ext_vector_type(16)));
typedef unsigned u32x4 __attribute__((ext_vector_type(4)));
typedef __attribute__((address_space(3))) const char* lds_cptr;
typedef short v4i16_t __attribute__((ext_vector_type(4)));

constexpr int SEQ = 2048, DH = 192, QB = 256, KT = 32, NW = 8;
constexpr int ROWB = DH * 2;
constexpr int TILEB = KT * ROWB;
constexpr int SLOTB = 2 * TILEB;
constexpr int NSLOT = 6, AHEAD = NSLOT - 1;
constexpr int LDS_BYTES = NSLOT * SLOTB;
constexpr float C2 = 0.07216878364870322f * 1.4426950408889634f;
constexpr float THR = 8.0f;

#define DI __device__ __forceinline__
#define MFMA(a, b, c) __builtin_amdgcn_mfma_f32_32x32x16_bf16(a, b, c, 0, 0, 0)

DI unsigned cvtpk(float lo, float hi) { unsigned r; asm("v_cvt_pk_bf16_f32 %0, %1, %2" : "=v"(r) : "v"(lo), "v"(hi)); return r; }
DI s16x4 vtr(lds_cptr p) { return __builtin_bit_cast(s16x4, __builtin_amdgcn_ds_read_tr16_b64_v4i16((__attribute__((address_space(3))) v4i16_t*)p)); }
DI bf16x8 ldsr128(lds_cptr p) { return *(const __attribute__((address_space(3))) bf16x8*)p; }

DI void dma3(const char* sbase, unsigned m0v, unsigned v0, unsigned v1, unsigned v2) {
    unsigned keep;
    asm volatile("s_mov_b32 %0, m0\n\ts_mov_b32 m0, %5\n\ts_nop 0\n\t"
                 "global_load_lds_dwordx4 %1, %4\n\t"
                 "global_load_lds_dwordx4 %2, %4 offset:1024\n\t"
                 "global_load_lds_dwordx4 %3, %4 offset:2048\n\t"
                 "s_mov_b32 m0, %0"
                 : "=&s"(keep) : "v"(v0), "v"(v1), "v"(v2), "s"(sbase), "s"(m0v) : "memory");
}
#define WB_CASE(N) case N: asm volatile("s_waitcnt vmcnt(" #N ") lgkmcnt(0)\n\ts_barrier" ::: "memory"); break;
DI void wait_bar(int n) {
    switch (n) {
        WB_CASE(3) WB_CASE(6) WB_CASE(9) WB_CASE(12) WB_CASE(15) WB_CASE(18) WB_CASE(21) WB_CASE(24)
        default: asm volatile("s_waitcnt vmcnt(0) lgkmcnt(0)\n\ts_barrier" ::: "memory"); break;
    }
}
#define MX3(a, b, c) __builtin_fmaxf(__builtin_fmaxf((a), (b)), (c))

__global__ __launch_bounds__(NW * 64, 2)
void swa_fwd_kernel(const char* __restrict__ Q, const char* __restrict__ K, const char* __restrict__ V,
                    const int* __restrict__ Wp, char* __restrict__ O, int heads_per_xcd) {
    __shared__ __attribute__((aligned(1024))) char lds[LDS_BYTES];
    const int tid = threadIdx.x, lane = tid & 63, c32 = lane & 31, hi = lane >> 5;
    const int wid = __builtin_amdgcn_readfirstlane(tid >> 6);
    const int bid = blockIdx.x;
    const int head = (bid & 7) * heads_per_xcd + (bid >> 6), jb = (bid >> 3) & 7;
    int W = *Wp;
    const size_t hoff = (size_t)head * SEQ * ROWB;
    const int q0 = jb * QB, qw = q0 + wid * 32;
    char* Ow = O + hoff + (size_t)qw * ROWB;

    if (W <= 0) {
        const __hip_bfloat16* Vh = (const __hip_bfloat16*)(V + hoff);
        if (tid < DH) {
            float acc = 0.f;
            for (int k = 0; k < SEQ; ++k) acc += __bfloat162float(Vh[(size_t)k * DH + tid]);
            const __hip_bfloat16 r = __float2bfloat16(acc * (1.0f / SEQ));
            __hip_bfloat16* Ob = (__hip_bfloat16*)(O + hoff + (size_t)q0 * ROWB);
            for (int q = 0; q < QB; ++q) Ob[(size_t)q * DH + tid] = r;
        }
        return;
    }
    if (W > SEQ) W = SEQ;

    const int t_hi = q0 / KT + NW - 1;
    const int lowk = q0 - W + 1, t_lo = lowk > 0 ? lowk / KT : 0;
    const int NT = t_hi - t_lo + 1;
    const bool rev = (jb & 1) && W <= QB;
    const int wt_hi = qw / KT, wl = qw - W + 1, wt_lo = wl > 0 ? wl / KT : 0;
    const int s_first = rev ? t_hi - wt_hi : wt_lo - t_lo, s_last = rev ? t_hi - wt_lo : wt_hi - t_lo;
    const int tstep = rev ? -1 : 1, t0 = rev ? t_hi : t_lo;

    const bool isV = wid >= 4;
    const int w4 = wid & 3;
    unsigned voff[3];
#pragma unroll
    for (int j = 0; j < 3; ++j) {
        const int rl = (8 * j + (lane >> 3)) / 3;
        const int f = isV ? (((rl >> 1) & 1) << 2) : ((4 * w4 + (rl >> 1)) & 7);
        voff[j] = (unsigned)((lane ^ f) * 16);
    }
    const char* src0 = (isV ? V : K) + hoff + (size_t)t0 * TILEB + w4 * 3072;
    const long sstep = (long)tstep * TILEB;
    const unsigned lds0 = (unsigned)(uintptr_t)lds;
    const unsigned dst0 = lds0 + (isV ? TILEB : 0) + w4 * 3072;
#define ISSUE(s_) dma3(src0 + (long)(s_) * sstep, dst0 + (unsigned)((s_) % NSLOT) * SLOTB, voff[0], voff[1], voff[2])

    const int npre = NT < AHEAD ? NT : AHEAD;
    const int qpos = s_first < npre - 1 ? s_first : npre - 1;
    for (int s = 0; s <= qpos; ++s) ISSUE(s);
    bf16x8 qf[12];
    {
        const char* qp = Q + hoff + (size_t)qw * ROWB;
        const unsigned qo = (unsigned)(c32 * ROWB + hi * 16);
#define QLD(i) asm volatile("global_load_dwordx4 %0, %1, %2 offset:%3" : "=v"(qf[i]) : "v"(qo), "s"(qp), "i"((i) * 32) : "memory")
        QLD(0); QLD(1); QLD(2); QLD(3); QLD(4); QLD(5); QLD(6); QLD(7); QLD(8); QLD(9); QLD(10); QLD(11);
#undef QLD
    }
    for (int s = qpos + 1; s < npre; ++s) ISSUE(s);

#define INFL(s_) (3 * (((s_) + AHEAD - 1 < NT - 1 ? (s_) + AHEAD - 1 : NT - 1) - (s_)))
    int s = 0;
    for (; s < s_first; ++s) {
        wait_bar(INFL(s) + (s <= qpos ? 12 : 0));
        if (s + AHEAD < NT) ISSUE(s + AHEAD);
    }
    wait_bar(INFL(s));
    asm volatile("" : "+v"(qf[0]), "+v"(qf[1]), "+v"(qf[2]), "+v"(qf[3]), "+v"(qf[4]), "+v"(qf[5]),
                      "+v"(qf[6]), "+v"(qf[7]), "+v"(qf[8]), "+v"(qf[9]), "+v"(qf[10]), "+v"(qf[11]));

    unsigned kb[4];
#pragma unroll
    for (int i = 0; i < 4; ++i) kb[i] = lds0 + c32 * ROWB + 16 * ((2 * i + hi) ^ ((c32 >> 1) & 7));
    const int tq = (lane >> 2) & 3, tbit = tq >> 1;
    const unsigned vbase = lds0 + TILEB + (4 * hi + tq) * ROWB + ((lane >> 4) & 1) * 32 + (lane & 3) * 8;
    const unsigned vbe = vbase + 64 * tbit, vbo = vbase - 64 * tbit;

    f32x16 o[6];
#pragma unroll
    for (int b = 0; b < 6; ++b) o[b] = f32x16{};
    float mref = -1e30f, lsum = 0.f;
    const int qrow = qw + c32;

    for (;; ++s) {
        if (s + AHEAD < NT) ISSUE(s + AHEAD);
        const unsigned so = (unsigned)(s % NSLOT) * SLOTB;
        const int kbase = (t0 + s * tstep) * KT;
        f32x16 p = f32x16{};
#pragma unroll
        for (int kk = 0; kk < 12; ++kk) {
            const bf16x8 kf = ldsr128((lds_cptr)(uintptr_t)(kb[kk & 3] + so + (kk >> 2) * 128));
            p = MFMA(kf, qf[kk], p);
        }
        if (kbase + KT - 1 > qw || kbase <= qw + 31 - W) {
            const int dq = qrow - kbase - 4 * hi;
#pragma unroll
            for (int r = 0; r < 16; ++r) {
                const int c = (r & 3) + 8 * (r >> 2);
                if ((unsigned)(dq - c) >= (unsigned)W) p[r] = -__builtin_inff();
            }
        }
        float a = MX3(p[0], p[1], p[2]), b2 = MX3(p[3], p[4], p[5]);
        a = MX3(a, p[6], p[7]); b2 = MX3(b2, p[8], p[9]); a = MX3(a, p[10], p[11]); b2 = MX3(b2, p[12], p[13]);
        a = MX3(a, p[14], p[15]); a = __builtin_fmaxf(a, b2);
        { auto rr = __builtin_amdgcn_permlane32_swap(__float_as_uint(a), __float_as_uint(a), false, false);
          a = __builtin_fmaxf(__uint_as_float(rr[0]), __uint_as_float(rr[1])); }
        const float pm = a * C2;
        if (__builtin_expect(__any(pm - mref > THR), 0)) {
            const float mn = __builtin_fmaxf(mref, pm);
            const float al = __builtin_amdgcn_exp2f(mref - mn);
            mref = mn; lsum *= al;
#pragma unroll
            for (int b = 0; b < 6; ++b)
#pragma unroll
                for (int r = 0; r < 16; ++r) o[b][r] *= al;
        }
        const float nm = -mref;
        float ps = 0.f;
#pragma unroll
        for (int r = 0; r < 16; ++r) { p[r] = __builtin_amdgcn_exp2f(__builtin_fmaf(p[r], C2, nm)); ps += p[r]; }
        lsum += ps;
        u32x4 w0 = {cvtpk(p[0], p[1]), cvtpk(p[2], p[3]), cvtpk(p[4], p[5]), cvtpk(p[6], p[7])};
        u32x4 w1 = {cvtpk(p[8], p[9]), cvtpk(p[10], p[11]), cvtpk(p[12], p[13]), cvtpk(p[14], p[15])};
        const bf16x8 pf0 = __builtin_bit_cast(bf16x8, w0), pf1 = __builtin_bit_cast(bf16x8, w1);
#pragma unroll
        for (int b = 0; b < 6; ++b) {
            const unsigned vb = ((b & 1) ? vbo : vbe) + so + 64 * b;
            const s16x4 a0 = vtr((lds_cptr)(uintptr_t)(vb)), a1 = vtr((lds_cptr)(uintptr_t)(vb + 8 * ROWB));
            const s16x4 a2 = vtr((lds_cptr)(uintptr_t)(vb + 16 * ROWB)), a3 = vtr((lds_cptr)(uintptr_t)(vb + 24 * ROWB));
            o[b] = MFMA(((bf16x8){a0[0], a0[1], a0[2], a0[3], a1[0], a1[1], a1[2], a1[3]}), pf0, o[b]);
            o[b] = MFMA(((bf16x8){a2[0], a2[1], a2[2], a2[3], a3[0], a3[1], a3[2], a3[3]}), pf1, o[b]);
        }
        if (s + 1 < NT) wait_bar(INFL(s + 1));
        if (s == s_last) break;
    }
    ++s;
    if (s < NT && s + AHEAD < NT) ISSUE(s + AHEAD);
    {
        auto rr = __builtin_amdgcn_permlane32_swap(__float_as_uint(lsum), __float_as_uint(lsum), false, false);
        const float rl = 1.0f / (__uint_as_float(rr[0]) + __uint_as_float(rr[1]));
        char* orow = Ow + c32 * ROWB + hi * 16;
#pragma unroll
        for (int b = 0; b < 6; ++b)
#pragma unroll
            for (int g = 0; g < 4; g += 2) {
                unsigned x0 = cvtpk(o[b][4 * g + 0] * rl, o[b][4 * g + 1] * rl), x1 = cvtpk(o[b][4 * g + 2] * rl, o[b][4 * g + 3] * rl);
                unsigned y0 = cvtpk(o[b][4 * g + 4] * rl, o[b][4 * g + 5] * rl), y1 = cvtpk(o[b][4 * g + 6] * rl, o[b][4 * g + 7] * rl);
                auto r0 = __builtin_amdgcn_permlane32_swap(x0, y0, false, false);
                auto r1 = __builtin_amdgcn_permlane32_swap(x1, y1, false, false);
                u32x4 w = {r0[0], r1[0], r0[1], r1[1]};
                *(u32x4*)(orow + b * 64 + g * 16) = w;
            }
    }
    const int s_st = s;
    for (++s; s < NT; ++s) {
        wait_bar(INFL(s) + (s <= s_st + AHEAD ? 12 : 0));
        if (s + AHEAD < NT) ISSUE(s + AHEAD);
    }
}

extern "C" void kernel_launch(void* const* d_in, const int* in_sizes, int n_in,
                              void* d_out, int out_size, void* d_ws, size_t ws_size,
                              hipStream_t stream) {
    (void)n_in; (void)d_ws; (void)ws_size; (void)out_size;
    const int heads = in_sizes[0] / (SEQ * DH);
    swa_fwd_kernel<<<dim3(heads * (SEQ / QB)), dim3(NW * 64), 0, stream>>>(
        (const char*)d_in[0], (const char*)d_in[1], (const char*)d_in[2], (const int*)d_in[3], (char*)d_out, heads / 8);
}
```

```cpp
#include <hip/hip_runtime.h>
#include <hip/hip_bf16.h>
#include <stdint.h>

#define KV_POL ""
#define Q_POL " nt"
#define O_POL " nt"

constexpr int SEQ = 2048, DH = 192, QB = 256, NW = 8;
constexpr int ROWB = DH * 2;
constexpr int LDS_BYTES = 131072 + 2 * 12288;

__global__ __launch_bounds__(NW * 64, 2)
void swa_fwd_kernel(const char* __restrict__ Q, const char* __restrict__ K, const char* __restrict__ V,
                    const int* __restrict__ Wp, char* __restrict__ O, int heads_per_xcd
                    ) {
    __shared__ __attribute__((aligned(1024))) char lds_raw[LDS_BYTES + 1024];
    int W, tid = threadIdx.x;
    asm volatile(
    "s_load_dwordx8 s[80:87], %2, 0x0\n\t"
    "s_load_dwordx2 s[88:89], %2, 0x20\n\t"
    "s_load_dword s90, %2, 0x28\n\t"
    "s_getpc_b64 s[64:65]\n\t"
    ".Lpf%=:\n\t"
    "v_lshlrev_b32_e32 v154, 6, v255\n\t"
    "v_min_u32_e32 v154, .Lend%=-.Lpf%=-4, v154\n\t"
    "global_load_dword v208, v154, s[64:65]\n\t"
    "v_lshrrev_b32_e32 v154, 6, v255\n\t"
    "v_and_b32_e32 v152, 63, v255\n\t"
    "v_readfirstlane_b32 s78, v154\n\t"
    "v_lshrrev_b32_e32 v153, 3, v152\n\t"
    "s_movk_i32 s66, 256\n\t"
    "s_bfe_u32 s35, %3, 0x30003\n\t"
    "s_lshl_b32 s35, s35, 3\n\t"
    "s_and_b32 s58, s78, 1\n\t"
    "s_lshl_b32 s58, s58, 6\n\t"
    "s_cmp_ge_u32 s78, 4\n\t"
    "s_cselect_b64 vcc, -1, 0\n\t"
    "s_cselect_b32 s42, 3, 5\n\t"
    "s_cselect_b32 s43, 0x10000, 0\n\t"
    "v_add_u32_e32 v154, 0, v153\n\t"
    "v_mul_u32_u24_e32 v154, 43, v154\n\t"
    "v_lshrrev_b32_e32 v154, 8, v154\n\t"
    "v_xor_b32_e32 v155, v152, v154\n\t"
    "v_lshlrev_b32_e32 v144, 4, v155\n\t"
    "v_xor_b32_e32 v148, s58, v144\n\t"
    "v_and_b32_e32 v154, 1, v154\n\t"
    "v_lshlrev_b32_e32 v154, 2, v154\n\t"
    "v_xor_b32_e32 v154, v152, v154\n\t"
    "v_lshlrev_b32_e32 v154, 4, v154\n\t"
    "v_cndmask_b32_e32 v237, v148, v154, vcc\n\t"
    "v_add_u32_e32 v154, 8, v153\n\t"
    "v_mul_u32_u24_e32 v154, 43, v154\n\t"
    "v_lshrrev_b32_e32 v154, 8, v154\n\t"
    "v_xor_b32_e32 v155, v152, v154\n\t"
    "v_lshlrev_b32_e32 v145, 4, v155\n\t"
    "v_xor_b32_e32 v149, s58, v145\n\t"
    "v_and_b32_e32 v154, 1, v154\n\t"
    "v_lshlrev_b32_e32 v154, 2, v154\n\t"
    "v_xor_b32_e32 v154, v152, v154\n\t"
    "v_lshlrev_b32_e32 v154, 4, v154\n\t"
    "v_cndmask_b32_e32 v238, v149, v154, vcc\n\t"
    "v_add_u32_e32 v154, 16, v153\n\t"
    "v_mul_u32_u24_e32 v154, 43, v154\n\t"
    "v_lshrrev_b32_e32 v154, 8, v154\n\t"
    "v_xor_b32_e32 v155, v152, v154\n\t"
    "v_lshlrev_b32_e32 v146, 4, v155\n\t"
    "v_xor_b32_e32 v150, s58, v146\n\t"
    "v_and_b32_e32 v154, 1, v154\n\t"
    "v_lshlrev_b32_e32 v154, 2, v154\n\t"
    "v_xor_b32_e32 v154, v152, v154\n\t"
    "v_lshlrev_b32_e32 v154, 4, v154\n\t"
    "v_cndmask_b32_e32 v239, v150, v154, vcc\n\t"
    "s_and_b32 s57, s78, 3\n\t"
    "s_mulk_i32 s57, 0xc00\n\t"
    "s_add_u32 s43, s43, s57\n\t"
    "s_add_i32 s58, s35, s78\n\t"
    "s_lshl_b32 s58, s58, 5\n\t"
    "v_and_b32_e32 v156, 31, v152\n\t"
    "v_lshrrev_b32_e32 v157, 5, v152\n\t"
    "v_lshlrev_b32_e32 v247, 2, v157\n\t"
    "v_sub_u32_e32 v246, v156, v247\n\t"
    "v_add_u32_e32 v246, s58, v246\n\t"
    "v_bfe_u32 v248, v156, 1, 3\n\t"
    "v_xor_b32_e32 v248, v248, v157\n\t"
    "v_mul_u32_u24_e32 v234, 0x180, v156\n\t"
    "v_lshl_add_u32 v234, v248, 4, v234\n\t"
    "v_bfe_u32 v248, v152, 2, 2\n\t"
    "v_add_u32_e32 v247, v247, v248\n\t"
    "v_mul_u32_u24_e32 v233, 0x180, v247\n\t"
    "v_bfe_u32 v247, v152, 4, 1\n\t"
    "v_lshl_add_u32 v233, v247, 5, v233\n\t"
    "v_and_b32_e32 v247, 3, v152\n\t"
    "v_lshl_add_u32 v233, v247, 3, v233\n\t"
    "v_lshrrev_b32_e32 v248, 1, v248\n\t"
    "v_lshl_add_u32 v245, v248, 6, v233\n\t"
    ".Lsetup%=:\n\t"
    "s_min_i32 s55, s66, 0x800\n\t"
    "s_lshl_b32 s58, s35, 5\n\t"
    "s_sub_i32 s58, s58, s55\n\t"
    "s_add_i32 s58, s58, 1\n\t"
    "s_lshr_b32 s34, s58, 31\n\t"
    "s_bfe_u32 s59, s35, 0x10003\n\t"
    "s_or_b32 s34, s34, s59\n\t"
    "s_lshl_b32 s59, s78, 5\n\t"
    "s_add_i32 s59, s59, s58\n\t"
    "s_max_i32 s58, s58, 0\n\t"
    "s_lshr_b32 s58, s58, 5\n\t"
    "s_max_i32 s59, s59, 0\n\t"
    "s_lshr_b32 s91, s59, 5\n\t"
    "s_add_i32 s59, s35, 7\n\t"
    "s_sub_i32 s41, s59, s58\n\t"
    "s_add_i32 s41, s41, 1\n\t"
    "s_sub_i32 s69, s91, s58\n\t"
    "s_add_i32 s47, s35, s78\n\t"
    "s_sub_i32 s47, s47, s58\n\t"
    "s_sub_i32 s65, 7, s78\n\t"
    "s_sub_i32 s60, s59, s91\n\t"
    "s_cmp_lg_u32 s34, 0\n\t"
    "s_cselect_b32 s69, s65, s69\n\t"
    "s_cselect_b32 s47, s60, s47\n\t"
    "s_cselect_b32 s79, s59, s58\n\t"
    "s_cselect_b32 s38, -1, 1\n\t"
    "s_cselect_b32 s59, s59, s35\n\t"
    "s_lshl_b32 s54, s38, 5\n\t"
    "s_ashr_i32 s39, s38, 31\n\t"
    "s_mulk_i32 s38, 0x3000\n\t"
    "s_mul_i32 s58, s79, 0x3000\n\t"
    "s_add_u32 s58, s58, s57\n\t"
    "s_mul_i32 s59, s59, 0x3000\n\t"
    "s_add_u32 s59, s59, s57\n\t"
    "s_mov_b32 s44, 0\n\t"
    "s_movk_i32 s45, 0x3000\n\t"
    "s_mov_b32 s40, 0\n\t"
    "s_and_b32 s33, %3, 7\n\t"
    "s_lshr_b32 s65, %3, 6\n\t"
    "v_mov_b32_e32 v0, 0\n\t"
    "v_mov_b32_e32 v1, 0\n\t"
    "v_mov_b32_e32 v2, 0\n\t"
    "v_mov_b32_e32 v3, 0\n\t"
    "v_mov_b32_e32 v4, 0\n\t"
    "v_mov_b32_e32 v5, 0\n\t"
    "v_mov_b32_e32 v6, 0\n\t"
    "v_mov_b32_e32 v7, 0\n\t"
    "v_mov_b32_e32 v8, 0\n\t"
    "v_mov_b32_e32 v9, 0\n\t"
    "v_mov_b32_e32 v10, 0\n\t"
    "v_mov_b32_e32 v11, 0\n\t"
    "v_mov_b32_e32 v12, 0\n\t"
    "v_mov_b32_e32 v13, 0\n\t"
    "v_mov_b32_e32 v14, 0\n\t"
    "v_mov_b32_e32 v15, 0\n\t"
    "v_mov_b32_e32 v16, 0\n\t"
    "v_mov_b32_e32 v17, 0\n\t"
    "v_mov_b32_e32 v18, 0\n\t"
    "v_mov_b32_e32 v19, 0\n\t"
    "v_mov_b32_e32 v20, 0\n\t"
    "v_mov_b32_e32 v21, 0\n\t"
    "v_mov_b32_e32 v22, 0\n\t"
    "v_mov_b32_e32 v23, 0\n\t"
    "v_mov_b32_e32 v24, 0\n\t"
    "v_mov_b32_e32 v25, 0\n\t"
    "v_mov_b32_e32 v26, 0\n\t"
    "v_mov_b32_e32 v27, 0\n\t"
    "v_mov_b32_e32 v28, 0\n\t"
    "v_mov_b32_e32 v29, 0\n\t"
    "v_mov_b32_e32 v30, 0\n\t"
    "v_mov_b32_e32 v31, 0\n\t"
    "v_mov_b32_e32 v32, 0\n\t"
    "v_mov_b32_e32 v33, 0\n\t"
    "v_mov_b32_e32 v34, 0\n\t"
    "v_mov_b32_e32 v35, 0\n\t"
    "v_mov_b32_e32 v36, 0\n\t"
    "v_mov_b32_e32 v37, 0\n\t"
    "v_mov_b32_e32 v38, 0\n\t"
    "v_mov_b32_e32 v39, 0\n\t"
    "v_mov_b32_e32 v40, 0\n\t"
    "v_mov_b32_e32 v41, 0\n\t"
    "v_mov_b32_e32 v42, 0\n\t"
    "v_mov_b32_e32 v43, 0\n\t"
    "v_mov_b32_e32 v44, 0\n\t"
    "v_mov_b32_e32 v45, 0\n\t"
    "v_mov_b32_e32 v46, 0\n\t"
    "v_mov_b32_e32 v47, 0\n\t"
    "s_cmp_gt_i32 s41, 8\n\t"
    "s_cbranch_scc1 .Lnodelay%=\n\t"
    "s_sleep 16\n\t"
    "s_sleep 16\n\t"
    "s_sleep 16\n\t"
    "s_sleep 16\n\t"
    "s_sleep 16\n\t"
    "s_sleep 16\n\t"
    "s_sleep 16\n\t"
    "s_sleep 16\n\t"
    "s_sleep 16\n\t"
    "s_sleep 16\n\t"
    "s_sleep 16\n\t"
    "s_sleep 16\n\t"
    ".Lnodelay%=:\n\t"
    "s_waitcnt lgkmcnt(0)\n\t"
    "s_load_dword s61, s[86:87], 0x0\n\t"
    "s_mul_i32 s33, s33, s90\n\t"
    "s_add_i32 s33, s33, s65\n\t"
    "s_mul_i32 s33, s33, 0xc0000\n\t"
    "s_add_u32 s58, s58, s33\n\t"
    "s_add_u32 s59, s59, s33\n\t"
    "s_cmp_ge_u32 s78, 4\n\t"
    "s_cselect_b64 s[36:37], s[84:85], s[82:83]\n\t"
    "s_cbranch_scc0 .Lnoq%=\n\t"
    "s_add_u32 s98, s80, s59\n\t"
    "s_addc_u32 s99, s81, 0\n\t"
    "s_add_u32 m0, s57, 0x20000\n\t"
    "s_nop 0\n\t"
    "global_load_lds_dwordx4 v148, s[98:99]" Q_POL "\n\t"
    "global_load_lds_dwordx4 v149, s[98:99] offset:1024" Q_POL "\n\t"
    "global_load_lds_dwordx4 v150, s[98:99] offset:2048" Q_POL "\n\t"
    "s_add_u32 s98, s98, s38\n\t"
    "s_addc_u32 s99, s99, s39\n\t"
    "s_add_u32 m0, s57, 0x23000\n\t"
    "s_nop 0\n\t"
    "global_load_lds_dwordx4 v148, s[98:99]" Q_POL "\n\t"
    "global_load_lds_dwordx4 v149, s[98:99] offset:1024" Q_POL "\n\t"
    "global_load_lds_dwordx4 v150, s[98:99] offset:2048" Q_POL "\n\t"
    ".Lnoq%=:\n\t"
    "s_add_u32 s36, s36, s58\n\t"
    "s_addc_u32 s37, s37, 0\n\t"
    "s_sub_i32 s60, s42, 3\n\t"
    ".Lisp%=:\n\t"
    "s_cmp_lt_i32 s40, s60\n\t"
    "s_cbranch_scc0 .Lisdp%=\n\t"
    "s_add_u32 m0, s43, s44\n\t"
    "s_xor_b32 s44, s44, s45\n\t"
    "global_load_lds_dwordx4 v237, s[36:37]" KV_POL "\n\t"
    "global_load_lds_dwordx4 v238, s[36:37] offset:1024" KV_POL "\n\t"
    "global_load_lds_dwordx4 v239, s[36:37] offset:2048" KV_POL "\n\t"
    "s_xor_b32 s45, s45, 0x8000\n\t"
    "s_add_u32 s36, s36, s38\n\t"
    "s_addc_u32 s37, s37, s39\n\t"
    "s_add_i32 s40, s40, 1\n\t"
    "s_branch .Lisp%=\n\t"
    ".Lisdp%=:\n\t"
    "s_add_i32 s58, s35, s78\n\t"
    "s_mul_i32 s59, s58, 0x3000\n\t"
    "s_add_u32 s59, s59, s33\n\t"
    "s_add_u32 s98, s80, s59\n\t"
    "s_addc_u32 s99, s81, 0\n\t"
    "s_add_u32 s74, s88, s59\n\t"
    "s_addc_u32 s75, s89, 0\n\t"
    "s_lshl_b32 s58, s58, 5\n\t"
    "s_bitcmp1_b32 s69, 0\n\t"
    "s_cselect_b32 s48, 0x3000, 0\n\t"
    "s_lshl_b32 s65, s69, 14\n\t"
    "s_and_b32 s65, s65, 0x8000\n\t"
    "s_or_b32 s48, s48, s65\n\t"
    "s_add_u32 s59, s48, 0x10000\n\t"
    "v_add_u32_e32 v232, s59, v245\n\t"
    "v_xor_b32_e32 v233, 64, v232\n\t"
    "s_bitcmp1_b32 s69, 0\n\t"
    "s_cselect_b32 s49, 0xb000, 0\n\t"
    "s_cselect_b32 s56, 0x3000, 0\n\t"
    "s_or_b32 s49, s49, 0x3000\n\t"
    "s_add_u32 s56, s56, 0x20000\n\t"
    "s_sub_i32 s58, s58, s55\n\t"
    "s_add_i32 s58, s58, 31\n\t"
    "s_ashr_i32 s59, s58, 5\n\t"
    "s_sub_i32 s59, s59, s91\n\t"
    "s_add_i32 s59, s59, 1\n\t"
    "s_cmp_lt_i32 s58, 0\n\t"
    "s_cselect_b32 s59, 0, s59\n\t"
    "s_cmp_lg_u32 s34, 0\n\t"
    "s_cselect_b32 s58, 1, s59\n\t"
    "s_cselect_b32 s59, s59, 1\n\t"
    "s_add_i32 s50, s69, s58\n\t"
    "s_sub_i32 s51, s47, s59\n\t"
    "s_sub_i32 s51, s51, s50\n\t"
    "s_add_i32 s58, s47, 5\n\t"
    "s_cmp_lt_i32 s51, 0\n\t"
    "s_cselect_b32 s50, s58, s50\n\t"
    "s_cselect_b32 s51, 0, s51\n\t"
    "s_sub_i32 s50, s50, 1\n\t"
    "s_lshl_b32 s53, s79, 5\n\t"
    "s_mov_b32 s52, 0x3dd53b95\n\t"
    "s_mov_b32 s62, 0x4119ac27\n\t"
    "s_mov_b32 s63, 0xff800000\n\t"
    "s_mul_i32 s67, s47, s54\n\t"
    "s_add_i32 s67, s67, s53\n\t"
    "s_add_i32 s53, s53, s54\n\t"
    "s_sub_i32 s53, s53, 1\n\t"
    "v_mov_b32_e32 v192, 0\n\t"
    "v_mov_b32_e32 v193, 0\n\t"
    "v_mov_b32_e32 v194, 0\n\t"
    "v_mov_b32_e32 v195, 0\n\t"
    "v_mov_b32_e32 v196, 0\n\t"
    "v_mov_b32_e32 v197, 0\n\t"
    "v_mov_b32_e32 v198, 0\n\t"
    "v_mov_b32_e32 v199, 0\n\t"
    "v_mov_b32_e32 v200, 0\n\t"
    "v_mov_b32_e32 v201, 0\n\t"
    "v_mov_b32_e32 v202, 0\n\t"
    "v_mov_b32_e32 v203, 0\n\t"
    "v_mov_b32_e32 v204, 0\n\t"
    "v_mov_b32_e32 v205, 0\n\t"
    "v_mov_b32_e32 v206, 0\n\t"
    "v_mov_b32_e32 v207, 0\n\t"
    "s_sub_i32 s46, s69, 1\n\t"
    "s_call_b64 s[76:77], .Lmask%=\n\t"
    "s_lshr_b32 s59, s78, 1\n\t"
    "s_lshl_b32 s59, s59, 15\n\t"
    "s_and_b32 s65, s78, 1\n\t"
    "s_lshl_b32 s65, s65, 12\n\t"
    "s_add_u32 s59, s59, s65\n\t"
    "s_add_u32 s59, s59, 0x6000\n\t"
    "v_bfe_u32 v247, v156, 3, 1\n\t"
    "v_xor_b32_e32 v247, v247, v157\n\t"
    "v_lshlrev_b32_e32 v247, 3, v247\n\t"
    "v_lshl_add_u32 v250, v156, 7, v247\n\t"
    "v_and_b32_e32 v247, 7, v152\n\t"
    "v_lshl_add_u32 v250, v247, 4, v250\n\t"
    "v_add_u32_e32 v250, s59, v250\n\t"
    "v_xor_b32_e32 v248, v247, v153\n\t"
    "v_lshlrev_b32_e32 v248, 4, v248\n\t"
    "v_lshl_add_u32 v253, v153, 7, v248\n\t"
    "v_add_u32_e32 v253, s59, v253\n\t"
    "v_mul_u32_u24_e32 v254, 0x180, v153\n\t"
    "v_lshl_add_u32 v254, v247, 4, v254\n\t"
    "v_subrev_u32_e32 v247, s67, v246\n\t"
    "v_subrev_u32_e32 v248, 0, v247\n\t"
    "v_cmp_gt_u32_e64 s[4:5], s55, v248\n\t"
    "v_subrev_u32_e32 v249, 1, v247\n\t"
    "v_cmp_gt_u32_e64 s[6:7], s55, v249\n\t"
    "v_subrev_u32_e32 v248, 2, v247\n\t"
    "v_cmp_gt_u32_e64 s[8:9], s55, v248\n\t"
    "v_subrev_u32_e32 v249, 3, v247\n\t"
    "v_cmp_gt_u32_e64 s[10:11], s55, v249\n\t"
    "v_subrev_u32_e32 v248, 8, v247\n\t"
    "v_cmp_gt_u32_e64 s[12:13], s55, v248\n\t"
    "v_subrev_u32_e32 v249, 9, v247\n\t"
    "v_cmp_gt_u32_e64 s[14:15], s55, v249\n\t"
    "v_subrev_u32_e32 v248, 10, v247\n\t"
    "v_cmp_gt_u32_e64 s[16:17], s55, v248\n\t"
    "v_subrev_u32_e32 v249, 11, v247\n\t"
    "v_cmp_gt_u32_e64 s[18:19], s55, v249\n\t"
    "v_subrev_u32_e32 v248, 16, v247\n\t"
    "v_cmp_gt_u32_e64 s[20:21], s55, v248\n\t"
    "v_subrev_u32_e32 v249, 17, v247\n\t"
    "v_cmp_gt_u32_e64 s[22:23], s55, v249\n\t"
    "v_subrev_u32_e32 v248, 18, v247\n\t"
    "v_cmp_gt_u32_e64 s[24:25], s55, v248\n\t"
    "v_subrev_u32_e32 v249, 19, v247\n\t"
    "v_cmp_gt_u32_e64 s[26:27], s55, v249\n\t"
    "v_subrev_u32_e32 v248, 24, v247\n\t"
    "v_cmp_gt_u32_e64 s[28:29], s55, v248\n\t"
    "v_subrev_u32_e32 v249, 25, v247\n\t"
    "v_cmp_gt_u32_e64 s[30:31], s55, v249\n\t"
    "v_subrev_u32_e32 v248, 26, v247\n\t"
    "v_cmp_gt_u32_e64 s[94:95], s55, v248\n\t"
    "v_subrev_u32_e32 v249, 27, v247\n\t"
    "v_cmp_gt_u32_e64 s[96:97], s55, v249\n\t"
    "v_mov_b32_e32 v48, 0\n\t"
    "v_mov_b32_e32 v49, 0\n\t"
    "v_mov_b32_e32 v50, 0\n\t"
    "v_mov_b32_e32 v51, 0\n\t"
    "v_mov_b32_e32 v52, 0\n\t"
    "v_mov_b32_e32 v53, 0\n\t"
    "v_mov_b32_e32 v54, 0\n\t"
    "v_mov_b32_e32 v55, 0\n\t"
    "v_mov_b32_e32 v56, 0\n\t"
    "v_mov_b32_e32 v57, 0\n\t"
    "v_mov_b32_e32 v58, 0\n\t"
    "v_mov_b32_e32 v59, 0\n\t"
    "v_mov_b32_e32 v60, 0\n\t"
    "v_mov_b32_e32 v61, 0\n\t"
    "v_mov_b32_e32 v62, 0\n\t"
    "v_mov_b32_e32 v63, 0\n\t"
    "v_mov_b32_e32 v64, 0\n\t"
    "v_mov_b32_e32 v65, 0\n\t"
    "v_mov_b32_e32 v66, 0\n\t"
    "v_mov_b32_e32 v67, 0\n\t"
    "v_mov_b32_e32 v68, 0\n\t"
    "v_mov_b32_e32 v69, 0\n\t"
    "v_mov_b32_e32 v70, 0\n\t"
    "v_mov_b32_e32 v71, 0\n\t"
    "v_mov_b32_e32 v72, 0\n\t"
    "v_mov_b32_e32 v73, 0\n\t"
    "v_mov_b32_e32 v74, 0\n\t"
    "v_mov_b32_e32 v75, 0\n\t"
    "v_mov_b32_e32 v76, 0\n\t"
    "v_mov_b32_e32 v77, 0\n\t"
    "v_mov_b32_e32 v78, 0\n\t"
    "v_mov_b32_e32 v79, 0\n\t"
    "v_mov_b32_e32 v80, 0\n\t"
    "v_mov_b32_e32 v81, 0\n\t"
    "v_mov_b32_e32 v82, 0\n\t"
    "v_mov_b32_e32 v83, 0\n\t"
    "v_mov_b32_e32 v84, 0\n\t"
    "v_mov_b32_e32 v85, 0\n\t"
    "v_mov_b32_e32 v86, 0\n\t"
    "v_mov_b32_e32 v87, 0\n\t"
    "v_mov_b32_e32 v88, 0\n\t"
    "v_mov_b32_e32 v89, 0\n\t"
    "v_mov_b32_e32 v90, 0\n\t"
    "v_mov_b32_e32 v91, 0\n\t"
    "v_mov_b32_e32 v92, 0\n\t"
    "v_mov_b32_e32 v93, 0\n\t"
    "v_mov_b32_e32 v94, 0\n\t"
    "v_mov_b32_e32 v95, 0\n\t"
    "v_mov_b32_e32 v242, 0\n\t"
    "v_mov_b32_e32 v243, 0\n\t"
    "s_waitcnt lgkmcnt(0)\n\t"
    "s_cmp_eq_u32 s61, s66\n\t"
    "s_cbranch_scc0 .Lrespec%=\n\t"
    "s_sub_i32 s60, s42, 2\n\t"
    ".Lisq%=:\n\t"
    "s_cmp_lt_i32 s40, s60\n\t"
    "s_cbranch_scc0 .Lisdq%=\n\t"
    "s_add_u32 m0, s43, s44\n\t"
    "s_xor_b32 s44, s44, s45\n\t"
    "global_load_lds_dwordx4 v237, s[36:37]" KV_POL "\n\t"
    "global_load_lds_dwordx4 v238, s[36:37] offset:1024" KV_POL "\n\t"
    "global_load_lds_dwordx4 v239, s[36:37] offset:2048" KV_POL "\n\t"
    "s_xor_b32 s45, s45, 0x8000\n\t"
    "s_add_u32 s36, s36, s38\n\t"
    "s_addc_u32 s37, s37, s39\n\t"
    "s_add_i32 s40, s40, 1\n\t"
    "s_branch .Lisq%=\n\t"
    ".Lisdq%=:\n\t"
    "s_mov_b32 s86, 0\n\t"
    "s_sub_i32 s34, s41, 3\n\t"
    "s_mov_b32 s33, 5\n\t"
    "s_sub_i32 s91, s41, s42\n\t"
    "s_sub_i32 s91, s91, 5\n\t"
    "s_max_i32 s91, s91, 0\n\t"
    "s_mov_b32 s46, -2\n\t"
    "s_mov_b32 s67, -8\n\t"
    "s_mov_b32 s68, -8\n\t"
    "s_mov_b32 s57, 0x100\n\t"
    "s_cmp_lt_i32 s69, 2\n\t"
    "s_cbranch_scc1 .Lhaveq%=\n\t"
    "s_sub_i32 s79, s69, 2\n\t"
    "s_call_b64 s[76:77], .Lidle%=\n\t"
    "s_add_u32 m0, s56, 0\n\t"
    "s_nop 0\n\t"
    "global_load_lds_dwordx4 v144, s[98:99]" Q_POL "\n\t"
    "global_load_lds_dwordx4 v145, s[98:99] offset:1024" Q_POL "\n\t"
    "global_load_lds_dwordx4 v146, s[98:99] offset:2048" Q_POL "\n\t"
    "s_add_u32 s98, s98, 0xc00\n\t"
    "s_addc_u32 s99, s99, 0\n\t"
    "v_xor_b32_e32 v148, 64, v144\n\t"
    "v_xor_b32_e32 v149, 64, v145\n\t"
    "v_xor_b32_e32 v150, 64, v146\n\t"
    "s_add_u32 m0, s56, 3072\n\t"
    "s_nop 0\n\t"
    "global_load_lds_dwordx4 v148, s[98:99]" Q_POL "\n\t"
    "global_load_lds_dwordx4 v149, s[98:99] offset:1024" Q_POL "\n\t"
    "global_load_lds_dwordx4 v150, s[98:99] offset:2048" Q_POL "\n\t"
    "s_add_u32 s98, s98, 0xc00\n\t"
    "s_addc_u32 s99, s99, 0\n\t"
    "s_add_u32 m0, s56, 6144\n\t"
    "s_nop 0\n\t"
    "global_load_lds_dwordx4 v144, s[98:99]" Q_POL "\n\t"
    "global_load_lds_dwordx4 v145, s[98:99] offset:1024" Q_POL "\n\t"
    "global_load_lds_dwordx4 v146, s[98:99] offset:2048" Q_POL "\n\t"
    "s_add_u32 s98, s98, 0xc00\n\t"
    "s_addc_u32 s99, s99, 0\n\t"
    "s_add_u32 m0, s56, 9216\n\t"
    "s_nop 0\n\t"
    "global_load_lds_dwordx4 v148, s[98:99]" Q_POL "\n\t"
    "global_load_lds_dwordx4 v149, s[98:99] offset:1024" Q_POL "\n\t"
    "global_load_lds_dwordx4 v150, s[98:99] offset:2048" Q_POL "\n\t"
    "s_mov_b32 s57, s40\n\t"
    "s_mov_b32 s67, 0x100\n\t"
    ".Lhaveq%=:\n\t"
    "s_sub_i32 s79, s69, 1\n\t"
    "s_call_b64 s[76:77], .Lidle%=\n\t"
    "v_add_u32_e32 v235, s48, v234\n\t"
    "ds_read_b128 v[224:227], v235 offset:0\n\t"
    "ds_read_b128 v[144:147], v235 offset:128\n\t"
    "ds_read_b128 v[148:151], v235 offset:256\n\t"
    "v_xor_b32_e32 v236, 32, v235\n\t"
    "ds_read_b128 v[152:155], v236 offset:0\n\t"
    "ds_read_b128 v[156:159], v236 offset:128\n\t"
    "ds_read_b128 v[160:163], v236 offset:256\n\t"
    "v_xor_b32_e32 v236, 64, v235\n\t"
    "ds_read_b128 v[164:167], v236 offset:0\n\t"
    "ds_read_b128 v[168:171], v236 offset:128\n\t"
    "ds_read_b128 v[172:175], v236 offset:256\n\t"
    "v_xor_b32_e32 v236, 96, v235\n\t"
    "ds_read_b128 v[176:179], v236 offset:0\n\t"
    "ds_read_b128 v[180:183], v236 offset:128\n\t"
    "ds_read_b128 v[184:187], v236 offset:256\n\t"
    "v_add_u32_e32 v235, s56, v234\n\t"
    "s_sub_i32 s58, s40, s46\n\t"
    "s_add_i32 s58, s58, 2\n\t"
    "s_sub_i32 s58, s58, s42\n\t"
    "s_sub_i32 s59, s40, s57\n\t"
    "s_cmp_lt_i32 s59, 0\n\t"
    "s_cbranch_scc1 .Lq0%=\n\t"
    "s_cmp_lt_i32 s59, 0\n\t"
    "s_cbranch_scc1 .Lvlqd%=\n\t"
    "s_cmp_lt_i32 s59, 2\n\t"
    "s_cbranch_scc1 .Lvlq1%=\n\t"
    "s_waitcnt vmcnt(6)\n\t"
    "s_branch .Lvlqd%=\n\t"
    ".Lvlq1%=:\n\t"
    "s_waitcnt vmcnt(3)\n\t"
    "s_cmp_lt_i32 s59, 1\n\t"
    "s_cbranch_scc0 .Lvlqd%=\n\t"
    "s_waitcnt vmcnt(0)\n\t"
    ".Lvlqd%=:\n\t"
    "s_mov_b32 s58, -1\n\t"
    ".Lq0%=:\n\t"
    "ds_read_b128 v[96:99], v235 offset:0\n\t"
    "ds_read_b128 v[100:103], v235 offset:128\n\t"
    "ds_read_b128 v[104:107], v235 offset:256\n\t"
    "v_xor_b32_e32 v236, 32, v235\n\t"
    "ds_read_b128 v[108:111], v236 offset:0\n\t"
    "ds_read_b128 v[112:115], v236 offset:128\n\t"
    "ds_read_b128 v[116:119], v236 offset:256\n\t"
    "v_xor_b32_e32 v236, 64, v235\n\t"
    "ds_read_b128 v[120:123], v236 offset:0\n\t"
    "ds_read_b128 v[124:127], v236 offset:128\n\t"
    "ds_read_b128 v[128:131], v236 offset:256\n\t"
    "v_xor_b32_e32 v236, 96, v235\n\t"
    "ds_read_b128 v[132:135], v236 offset:0\n\t"
    "ds_read_b128 v[136:139], v236 offset:128\n\t"
    "ds_read_b128 v[140:143], v236 offset:256\n\t"
    "s_xor_b32 s48, s48, s49\n\t"
    "v_add_u32_e32 v235, s48, v234\n\t"
    "s_xor_b32 s59, s49, 0x8000\n\t"
    "s_xor_b32 s48, s48, s59\n\t"
    "s_cmp_eq_u32 s46, s34\n\t"
    "s_cselect_b32 s58, 0, s58\n\t"
    "s_cmp_lt_i32 s58, 0\n\t"
    "s_cbranch_scc1 .Lvlrd%=\n\t"
    "s_cmp_lt_i32 s58, 2\n\t"
    "s_cbranch_scc1 .Lvlr1%=\n\t"
    "s_waitcnt vmcnt(6)\n\t"
    "s_branch .Lvlrd%=\n\t"
    ".Lvlr1%=:\n\t"
    "s_waitcnt vmcnt(3)\n\t"
    "s_cmp_lt_i32 s58, 1\n\t"
    "s_cbranch_scc0 .Lvlrd%=\n\t"
    "s_waitcnt vmcnt(0)\n\t"
    ".Lvlrd%=:\n\t"
    "s_cmp_ge_i32 s46, 4\n\t"
    "s_addc_u32 s60, s46, s42\n\t"
    "s_min_i32 s60, s60, s41\n\t"
    "s_waitcnt lgkmcnt(0)\n\t"
    "s_cmp_gt_i32 s46, s34\n\t"
    "s_cbranch_scc1 .Lnb0%=\n\t"
    "s_barrier\n\t"
    ".Lnb0%=:\n\t"
    "v_mfma_f32_32x32x16_bf16 v[192:207], v[224:227], v[96:99], v[192:207]\n\t"
    "ds_read_b128 v[224:227], v235 offset:0\n\t"
    ".Lisa%=:\n\t"
    "s_cmp_lt_i32 s40, s60\n\t"
    "s_cbranch_scc0 .Lisda%=\n\t"
    "s_add_u32 m0, s43, s44\n\t"
    "s_xor_b32 s44, s44, s45\n\t"
    "global_load_lds_dwordx4 v237, s[36:37]" KV_POL "\n\t"
    "global_load_lds_dwordx4 v238, s[36:37] offset:1024" KV_POL "\n\t"
    "global_load_lds_dwordx4 v239, s[36:37] offset:2048" KV_POL "\n\t"
    "s_xor_b32 s45, s45, 0x8000\n\t"
    "s_add_u32 s36, s36, s38\n\t"
    "s_addc_u32 s37, s37, s39\n\t"
    "s_add_i32 s40, s40, 1\n\t"
    "s_branch .Lisa%=\n\t"
    ".Lisda%=:\n\t"
    "v_mfma_f32_32x32x16_bf16 v[192:207], v[144:147], v[100:103], v[192:207]\n\t"
    "ds_read_b128 v[144:147], v235 offset:128\n\t"
    "v_mfma_f32_32x32x16_bf16 v[192:207], v[148:151], v[104:107], v[192:207]\n\t"
    "ds_read_b128 v[148:151], v235 offset:256\n\t"
    "v_mfma_f32_32x32x16_bf16 v[192:207], v[152:155], v[108:111], v[192:207]\n\t"
    "v_xor_b32_e32 v236, 32, v235\n\t"
    "ds_read_b128 v[152:155], v236 offset:0\n\t"
    "v_mfma_f32_32x32x16_bf16 v[192:207], v[156:159], v[112:115], v[192:207]\n\t"
    "ds_read_b128 v[156:159], v236 offset:128\n\t"
    "v_mfma_f32_32x32x16_bf16 v[192:207], v[160:163], v[116:119], v[192:207]\n\t"
    "ds_read_b128 v[160:163], v236 offset:256\n\t"
    "v_mfma_f32_32x32x16_bf16 v[192:207], v[164:167], v[120:123], v[192:207]\n\t"
    "v_xor_b32_e32 v236, 64, v235\n\t"
    "ds_read_b128 v[164:167], v236 offset:0\n\t"
    "v_mfma_f32_32x32x16_bf16 v[192:207], v[168:171], v[124:127], v[192:207]\n\t"
    "ds_read_b128 v[168:171], v236 offset:128\n\t"
    "v_mfma_f32_32x32x16_bf16 v[192:207], v[172:175], v[128:131], v[192:207]\n\t"
    "ds_read_b128 v[172:175], v236 offset:256\n\t"
    "v_mfma_f32_32x32x16_bf16 v[192:207], v[176:179], v[132:135], v[192:207]\n\t"
    "v_xor_b32_e32 v236, 96, v235\n\t"
    "ds_read_b128 v[176:179], v236 offset:0\n\t"
    "v_mfma_f32_32x32x16_bf16 v[192:207], v[180:183], v[136:139], v[192:207]\n\t"
    "ds_read_b128 v[180:183], v236 offset:128\n\t"
    "v_mfma_f32_32x32x16_bf16 v[192:207], v[184:187], v[140:143], v[192:207]\n\t"
    "ds_read_b128 v[184:187], v236 offset:256\n\t"
    "s_add_i32 s46, s46, 1\n\t"
    "s_sub_i32 s58, s46, s33\n\t"
    "s_nop 9\n\t"
    "v_max3_f32 v244, v192, v193, v194\n\t"
    "v_max3_f32 v245, v195, v196, v197\n\t"
    "v_max3_f32 v244, v244, v198, v199\n\t"
    "v_max3_f32 v245, v245, v200, v201\n\t"
    "v_max3_f32 v244, v244, v202, v203\n\t"
    "v_max3_f32 v245, v245, v204, v205\n\t"
    "v_max3_f32 v244, v244, v206, v207\n\t"
    "v_max_f32_e32 v244, v244, v245\n\t"
    "v_mov_b32_e32 v247, v244\n\t"
    "s_nop 1\n\t"
    "v_permlane32_swap_b32_e32 v244, v247\n\t"
    "v_max_f32_e32 v244, v244, v247\n\t"
    "v_mul_f32_e32 v247, s52, v244\n\t"
    "v_cmp_neq_f32_e32 vcc, s63, v247\n\t"
    "v_add_f32_e32 v248, 0x41000000, v247\n\t"
    "v_mul_f32_e32 v241, s62, v248\n\t"
    "v_cndmask_b32_e32 v240, 0, v247, vcc\n\t"
    "v_fma_f32 v208, v192, s52, -v240\n\t"
    "v_fma_f32 v209, v193, s52, -v240\n\t"
    "v_fma_f32 v210, v194, s52, -v240\n\t"
    "v_fma_f32 v211, v195, s52, -v240\n\t"
    "v_exp_f32_e32 v208, v208\n\t"
    "v_exp_f32_e32 v209, v209\n\t"
    "v_exp_f32_e32 v210, v210\n\t"
    "v_exp_f32_e32 v211, v211\n\t"
    "v_fma_f32 v212, v196, s52, -v240\n\t"
    "v_fma_f32 v213, v197, s52, -v240\n\t"
    "v_fma_f32 v214, v198, s52, -v240\n\t"
    "v_fma_f32 v215, v199, s52, -v240\n\t"
    "v_fma_f32 v216, v200, s52, -v240\n\t"
    "v_fma_f32 v217, v201, s52, -v240\n\t"
    "v_fma_f32 v218, v202, s52, -v240\n\t"
    "v_fma_f32 v219, v203, s52, -v240\n\t"
    "v_fma_f32 v220, v204, s52, -v240\n\t"
    "v_fma_f32 v221, v205, s52, -v240\n\t"
    "v_fma_f32 v222, v206, s52, -v240\n\t"
    "v_fma_f32 v223, v207, s52, -v240\n\t"
    "s_branch .Ltail%=\n\t"
    ".p2align 6\n\t"
    ".Lloop%=:\n\t"
    "s_barrier\n\t"
    ".Lloopnb%=:\n\t"
    "v_mfma_f32_32x32x16_bf16 v[192:207], v[224:227], v[96:99], 0\n\t"
    "ds_read_b64_tr_b16 v[188:189], v233 offset:6400\n\t"
    "ds_read_b64_tr_b16 v[190:191], v233 offset:9472\n\t"
    "v_exp_f32_e32 v212, v212\n\t"
    "v_add_f32_e32 v242, v242, v208\n\t"
    "v_add_f32_e32 v243, v243, v209\n\t"
    "v_mfma_f32_32x32x16_bf16 v[192:207], v[144:147], v[100:103], v[192:207]\n\t"
    "ds_read_b64_tr_b16 v[144:145], v232 offset:0\n\t"
    "ds_read_b64_tr_b16 v[146:147], v232 offset:3072\n\t"
    "v_exp_f32_e32 v213, v213\n\t"
    "v_cvt_pk_bf16_f32 v224, v208, v209\n\t"
    "v_add_f32_e32 v242, v242, v210\n\t"
    "v_mfma_f32_32x32x16_bf16 v[192:207], v[148:151], v[104:107], v[192:207]\n\t"
    "ds_read_b64_tr_b16 v[148:149], v233 offset:0\n\t"
    "ds_read_b64_tr_b16 v[150:151], v233 offset:3072\n\t"
    "v_exp_f32_e32 v214, v214\n\t"
    "v_cvt_pk_bf16_f32 v225, v210, v211\n\t"
    "v_add_f32_e32 v243, v243, v211\n\t"
    "v_mfma_f32_32x32x16_bf16 v[192:207], v[152:155], v[108:111], v[192:207]\n\t"
    "ds_read_b64_tr_b16 v[152:153], v232 offset:128\n\t"
    "ds_read_b64_tr_b16 v[154:155], v232 offset:3200\n\t"
    "v_exp_f32_e32 v215, v215\n\t"
    "v_cvt_pk_bf16_f32 v226, v212, v213\n\t"
    "v_add_f32_e32 v242, v242, v212\n\t"
    "v_mfma_f32_32x32x16_bf16 v[192:207], v[156:159], v[112:115], v[192:207]\n\t"
    "ds_read_b64_tr_b16 v[156:157], v233 offset:128\n\t"
    "ds_read_b64_tr_b16 v[158:159], v233 offset:3200\n\t"
    "v_exp_f32_e32 v216, v216\n\t"
    "v_add_f32_e32 v243, v243, v213\n\t"
    "v_add_f32_e32 v242, v242, v214\n\t"
    "v_mfma_f32_32x32x16_bf16 v[192:207], v[160:163], v[116:119], v[192:207]\n\t"
    "ds_read_b64_tr_b16 v[160:161], v232 offset:256\n\t"
    "ds_read_b64_tr_b16 v[162:163], v232 offset:3328\n\t"
    "v_exp_f32_e32 v217, v217\n\t"
    "v_cvt_pk_bf16_f32 v227, v214, v215\n\t"
    "v_add_f32_e32 v243, v243, v215\n\t"
    "s_setprio 2\n\t"
    "v_mfma_f32_32x32x16_bf16 v[192:207], v[164:167], v[120:123], v[192:207]\n\t"
    "ds_read_b64_tr_b16 v[164:165], v233 offset:256\n\t"
    "ds_read_b64_tr_b16 v[166:167], v233 offset:3328\n\t"
    "v_exp_f32_e32 v218, v218\n\t"
    "v_cvt_pk_bf16_f32 v228, v216, v217\n\t"
    "v_add_f32_e32 v242, v242, v216\n\t"
    "v_mfma_f32_32x32x16_bf16 v[192:207], v[168:171], v[124:127], v[192:207]\n\t"
    "ds_read_b64_tr_b16 v[168:169], v232 offset:6144\n\t"
    "ds_read_b64_tr_b16 v[170:171], v232 offset:9216\n\t"
    "v_exp_f32_e32 v219, v219\n\t"
    "v_add_f32_e32 v243, v243, v217\n\t"
    "v_add_f32_e32 v242, v242, v218\n\t"
    "v_mfma_f32_32x32x16_bf16 v[192:207], v[172:175], v[128:131], v[192:207]\n\t"
    "ds_read_b64_tr_b16 v[172:173], v233 offset:6144\n\t"
    "ds_read_b64_tr_b16 v[174:175], v233 offset:9216\n\t"
    "v_exp_f32_e32 v220, v220\n\t"
    "v_cvt_pk_bf16_f32 v229, v218, v219\n\t"
    "v_add_f32_e32 v243, v243, v219\n\t"
    "v_mfma_f32_32x32x16_bf16 v[192:207], v[176:179], v[132:135], v[192:207]\n\t"
    "ds_read_b64_tr_b16 v[176:177], v232 offset:6272\n\t"
    "ds_read_b64_tr_b16 v[178:179], v232 offset:9344\n\t"
    "v_exp_f32_e32 v221, v221\n\t"
    "v_add_f32_e32 v242, v242, v220\n\t"
    "v_add_u32_e32 v235, s48, v234\n\t"
    "v_mfma_f32_32x32x16_bf16 v[192:207], v[180:183], v[136:139], v[192:207]\n\t"
    "ds_read_b64_tr_b16 v[180:181], v233 offset:6272\n\t"
    "ds_read_b64_tr_b16 v[182:183], v233 offset:9344\n\t"
    "v_exp_f32_e32 v222, v222\n\t"
    "v_cvt_pk_bf16_f32 v230, v220, v221\n\t"
    "v_add_f32_e32 v243, v243, v221\n\t"
    "v_mfma_f32_32x32x16_bf16 v[192:207], v[184:187], v[140:143], v[192:207]\n\t"
    "ds_read_b64_tr_b16 v[184:185], v232 offset:6400\n\t"
    "ds_read_b64_tr_b16 v[186:187], v232 offset:9472\n\t"
    "v_exp_f32_e32 v223, v223\n\t"
    "v_add_f32_e32 v242, v242, v222\n\t"
    "s_setprio 1\n\t"
    "s_waitcnt lgkmcnt(12)\n\t"
    "v_mfma_f32_32x32x16_bf16 v[0:15], v[144:147], v[224:227], v[0:15]\n\t"
    "ds_read_b128 v[144:147], v235 offset:128\n\t"
    "v_cvt_pk_bf16_f32 v231, v222, v223\n\t"
    "v_add_f32_e32 v243, v243, v223\n\t"
    "s_cmp_ge_i32 s46, 4\n\t"
    "s_addc_u32 s60, s46, s42\n\t"
    "s_min_i32 s60, s60, s41\n\t"
    "v_mfma_f32_32x32x16_bf16 v[16:31], v[148:151], v[224:227], v[16:31]\n\t"
    "ds_read_b128 v[148:151], v235 offset:256\n\t"
    ".Lisb%=:\n\t"
    "s_cmp_lt_i32 s40, s60\n\t"
    "s_cbranch_scc0 .Lisdb%=\n\t"
    "s_add_u32 m0, s43, s44\n\t"
    "s_xor_b32 s44, s44, s45\n\t"
    "global_load_lds_dwordx4 v237, s[36:37]" KV_POL "\n\t"
    "global_load_lds_dwordx4 v238, s[36:37] offset:1024" KV_POL "\n\t"
    "global_load_lds_dwordx4 v239, s[36:37] offset:2048" KV_POL "\n\t"
    "s_xor_b32 s45, s45, 0x8000\n\t"
    "s_add_u32 s36, s36, s38\n\t"
    "s_addc_u32 s37, s37, s39\n\t"
    "s_add_i32 s40, s40, 1\n\t"
    "s_branch .Lisb%=\n\t"
    ".Lisdb%=:\n\t"
    "v_mfma_f32_32x32x16_bf16 v[32:47], v[152:155], v[224:227], v[32:47]\n\t"
    "v_xor_b32_e32 v236, 32, v235\n\t"
    "ds_read_b128 v[152:155], v236 offset:0\n\t"
    "s_sub_i32 s58, s46, s50\n\t"
    "s_cmp_gt_u32 s58, s51\n\t"
    "s_cbranch_scc1 .Lmask1%=\n\t"
    ".Lmaskd1%=:\n\t"
    "v_max3_f32 v244, v192, v193, v194\n\t"
    "v_mfma_f32_32x32x16_bf16 v[48:63], v[156:159], v[224:227], v[48:63]\n\t"
    "ds_read_b128 v[156:159], v236 offset:128\n\t"
    "v_max3_f32 v245, v195, v196, v197\n\t"
    "v_fma_f32 v208, v192, s52, -v240\n\t"
    "v_fma_f32 v209, v193, s52, -v240\n\t"
    "v_fma_f32 v210, v194, s52, -v240\n\t"
    "v_fma_f32 v211, v195, s52, -v240\n\t"
    "v_mfma_f32_32x32x16_bf16 v[64:79], v[160:163], v[224:227], v[64:79]\n\t"
    "ds_read_b128 v[160:163], v236 offset:256\n\t"
    "v_max3_f32 v244, v244, v198, v199\n\t"
    "v_exp_f32_e32 v208, v208\n\t"
    "v_fma_f32 v212, v196, s52, -v240\n\t"
    "v_xor_b32_e32 v236, 64, v235\n\t"
    "s_waitcnt lgkmcnt(5)\n\t"
    "v_mfma_f32_32x32x16_bf16 v[80:95], v[164:167], v[224:227], v[80:95]\n\t"
    "ds_read_b128 v[164:167], v236 offset:0\n\t"
    "ds_read_b128 v[224:227], v235 offset:0\n\t"
    "v_max3_f32 v245, v245, v200, v201\n\t"
    "v_exp_f32_e32 v209, v209\n\t"
    "v_fma_f32 v213, v197, s52, -v240\n\t"
    "s_setprio 0\n\t"
    "v_mfma_f32_32x32x16_bf16 v[0:15], v[168:171], v[228:231], v[0:15]\n\t"
    "ds_read_b128 v[168:171], v236 offset:128\n\t"
    "v_max3_f32 v244, v244, v202, v203\n\t"
    "v_exp_f32_e32 v210, v210\n\t"
    "v_fma_f32 v214, v198, s52, -v240\n\t"
    "v_fma_f32 v215, v199, s52, -v240\n\t"
    "v_mfma_f32_32x32x16_bf16 v[16:31], v[172:175], v[228:231], v[16:31]\n\t"
    "ds_read_b128 v[172:175], v236 offset:256\n\t"
    "v_max3_f32 v245, v245, v204, v205\n\t"
    "v_exp_f32_e32 v211, v211\n\t"
    "v_fma_f32 v216, v200, s52, -v240\n\t"
    "v_xor_b32_e32 v236, 96, v235\n\t"
    "v_mfma_f32_32x32x16_bf16 v[32:47], v[176:179], v[228:231], v[32:47]\n\t"
    "ds_read_b128 v[176:179], v236 offset:0\n\t"
    "v_max3_f32 v244, v244, v206, v207\n\t"
    "v_fma_f32 v217, v201, s52, -v240\n\t"
    "v_fma_f32 v218, v202, s52, -v240\n\t"
    "v_fma_f32 v219, v203, s52, -v240\n\t"
    "v_fma_f32 v220, v204, s52, -v240\n\t"
    "v_mfma_f32_32x32x16_bf16 v[48:63], v[180:183], v[228:231], v[48:63]\n\t"
    "ds_read_b128 v[180:183], v236 offset:128\n\t"
    "v_max_f32_e32 v244, v244, v245\n\t"
    "v_fma_f32 v221, v205, s52, -v240\n\t"
    "v_fma_f32 v222, v206, s52, -v240\n\t"
    "v_fma_f32 v223, v207, s52, -v240\n\t"
    "s_add_i32 s46, s46, 1\n\t"
    "v_mfma_f32_32x32x16_bf16 v[64:79], v[184:187], v[228:231], v[64:79]\n\t"
    "ds_read_b128 v[184:187], v236 offset:256\n\t"
    "v_cmp_gt_f32_e32 vcc, v244, v241\n\t"
    "v_xor_b32_e32 v232, s49, v232\n\t"
    "v_xor_b32_e32 v233, s49, v233\n\t"
    "s_xor_b32 s48, s48, s49\n\t"
    "s_xor_b32 s49, s49, 0x8000\n\t"
    "v_mfma_f32_32x32x16_bf16 v[80:95], v[188:191], v[228:231], v[80:95]\n\t"
    "s_sub_i32 s58, s46, s33\n\t"
    "s_cbranch_vccnz .Lrare%=\n\t"
    ".Lrared%=:\n\t"
    ".Ltail%=:\n\t"
    "s_setprio 3\n\t"
    "s_cmp_lt_u32 s58, s91\n\t"
    "s_cbranch_scc0 .Lslow%=\n\t"
    "s_waitcnt vmcnt(6) lgkmcnt(0)\n\t"
    ".Lslowd%=:\n\t"
    "s_cmp_lt_i32 s46, s47\n\t"
    "s_cbranch_scc1 .Lloop%=\n\t"
    "s_barrier\n\t"
    ".Llast%=:\n\t"
    "ds_read_b64_tr_b16 v[144:145], v232 offset:0\n\t"
    "ds_read_b64_tr_b16 v[146:147], v232 offset:3072\n\t"
    "ds_read_b64_tr_b16 v[148:149], v233 offset:0\n\t"
    "ds_read_b64_tr_b16 v[150:151], v233 offset:3072\n\t"
    "ds_read_b64_tr_b16 v[152:153], v232 offset:128\n\t"
    "ds_read_b64_tr_b16 v[154:155], v232 offset:3200\n\t"
    "v_exp_f32_e32 v212, v212\n\t"
    "v_exp_f32_e32 v213, v213\n\t"
    "v_exp_f32_e32 v214, v214\n\t"
    "v_exp_f32_e32 v215, v215\n\t"
    "v_cvt_pk_bf16_f32 v224, v208, v209\n\t"
    "v_cvt_pk_bf16_f32 v225, v210, v211\n\t"
    "ds_read_b64_tr_b16 v[168:169], v232 offset:6144\n\t"
    "ds_read_b64_tr_b16 v[170:171], v232 offset:9216\n\t"
    "ds_read_b64_tr_b16 v[172:173], v233 offset:6144\n\t"
    "ds_read_b64_tr_b16 v[174:175], v233 offset:9216\n\t"
    "ds_read_b64_tr_b16 v[176:177], v232 offset:6272\n\t"
    "ds_read_b64_tr_b16 v[178:179], v232 offset:9344\n\t"
    "v_cvt_pk_bf16_f32 v226, v212, v213\n\t"
    "v_cvt_pk_bf16_f32 v227, v214, v215\n\t"
    "v_exp_f32_e32 v216, v216\n\t"
    "v_exp_f32_e32 v217, v217\n\t"
    "ds_read_b64_tr_b16 v[156:157], v233 offset:128\n\t"
    "ds_read_b64_tr_b16 v[158:159], v233 offset:3200\n\t"
    "ds_read_b64_tr_b16 v[180:181], v233 offset:6272\n\t"
    "ds_read_b64_tr_b16 v[182:183], v233 offset:9344\n\t"
    "s_waitcnt lgkmcnt(14)\n\t"
    "v_mfma_f32_32x32x16_bf16 v[0:15], v[144:147], v[224:227], v[0:15]\n\t"
    "ds_read_b64_tr_b16 v[160:161], v232 offset:256\n\t"
    "ds_read_b64_tr_b16 v[162:163], v232 offset:3328\n\t"
    "v_exp_f32_e32 v218, v218\n\t"
    "v_exp_f32_e32 v219, v219\n\t"
    "v_cvt_pk_bf16_f32 v228, v216, v217\n\t"
    "s_waitcnt lgkmcnt(14)\n\t"
    "v_mfma_f32_32x32x16_bf16 v[16:31], v[148:151], v[224:227], v[16:31]\n\t"
    "ds_read_b64_tr_b16 v[184:185], v232 offset:6400\n\t"
    "ds_read_b64_tr_b16 v[186:187], v232 offset:9472\n\t"
    "v_exp_f32_e32 v220, v220\n\t"
    "v_exp_f32_e32 v221, v221\n\t"
    "v_cvt_pk_bf16_f32 v229, v218, v219\n\t"
    "s_waitcnt lgkmcnt(14)\n\t"
    "v_mfma_f32_32x32x16_bf16 v[32:47], v[152:155], v[224:227], v[32:47]\n\t"
    "ds_read_b64_tr_b16 v[164:165], v233 offset:256\n\t"
    "ds_read_b64_tr_b16 v[166:167], v233 offset:3328\n\t"
    "v_exp_f32_e32 v222, v222\n\t"
    "v_exp_f32_e32 v223, v223\n\t"
    "v_cvt_pk_bf16_f32 v230, v220, v221\n\t"
    "ds_read_b64_tr_b16 v[188:189], v233 offset:6400\n\t"
    "ds_read_b64_tr_b16 v[190:191], v233 offset:9472\n\t"
    "v_add_f32_e32 v242, v242, v208\n\t"
    "v_cvt_pk_bf16_f32 v231, v222, v223\n\t"
    "v_add_f32_e32 v243, v243, v209\n\t"
    "v_add_f32_e32 v242, v242, v210\n\t"
    "s_waitcnt lgkmcnt(12)\n\t"
    "v_mfma_f32_32x32x16_bf16 v[0:15], v[168:171], v[228:231], v[0:15]\n\t"
    "v_add_f32_e32 v243, v243, v211\n\t"
    "v_add_f32_e32 v242, v242, v212\n\t"
    "v_add_f32_e32 v243, v243, v213\n\t"
    "v_add_f32_e32 v242, v242, v214\n\t"
    "v_add_f32_e32 v243, v243, v215\n\t"
    "v_add_f32_e32 v242, v242, v216\n\t"
    "v_mfma_f32_32x32x16_bf16 v[16:31], v[172:175], v[228:231], v[16:31]\n\t"
    "v_add_f32_e32 v243, v243, v217\n\t"
    "v_add_f32_e32 v242, v242, v218\n\t"
    "v_add_f32_e32 v243, v243, v219\n\t"
    "v_add_f32_e32 v242, v242, v220\n\t"
    "v_add_f32_e32 v243, v243, v221\n\t"
    "v_add_f32_e32 v242, v242, v222\n\t"
    "v_mfma_f32_32x32x16_bf16 v[32:47], v[176:179], v[228:231], v[32:47]\n\t"
    "v_add_f32_e32 v243, v243, v223\n\t"
    "v_add_f32_e32 v242, v242, v243\n\t"
    "v_mov_b32_e32 v243, v242\n\t"
    "s_cmp_ge_i32 s46, 4\n\t"
    "s_addc_u32 s60, s46, s42\n\t"
    "s_min_i32 s60, s60, s41\n\t"
    "s_waitcnt lgkmcnt(0)\n\t"
    "v_mfma_f32_32x32x16_bf16 v[48:63], v[156:159], v[224:227], v[48:63]\n\t"
    "v_permlane32_swap_b32_e32 v242, v243\n\t"
    "v_add_f32_e32 v242, v242, v243\n\t"
    "v_rcp_f32_e32 v249, v242\n\t"
    "v_xor_b32_e32 v245, 16, v250\n\t"
    "v_xor_b32_e32 v235, 32, v250\n\t"
    "v_xor_b32_e32 v236, 48, v250\n\t"
    "v_mfma_f32_32x32x16_bf16 v[48:63], v[180:183], v[228:231], v[48:63]\n\t"
    "v_mul_f32_e32 v0, v249, v0\n\t"
    "v_mul_f32_e32 v1, v249, v1\n\t"
    "v_mul_f32_e32 v2, v249, v2\n\t"
    "v_mul_f32_e32 v3, v249, v3\n\t"
    "v_cvt_pk_bf16_f32 v0, v0, v1\n\t"
    "v_cvt_pk_bf16_f32 v1, v2, v3\n\t"
    "ds_write_b64 v250, v[0:1]\n\t"
    "v_mfma_f32_32x32x16_bf16 v[64:79], v[160:163], v[224:227], v[64:79]\n\t"
    "v_mul_f32_e32 v4, v249, v4\n\t"
    "v_mul_f32_e32 v5, v249, v5\n\t"
    "v_mul_f32_e32 v6, v249, v6\n\t"
    "v_mul_f32_e32 v7, v249, v7\n\t"
    "v_cvt_pk_bf16_f32 v4, v4, v5\n\t"
    "v_cvt_pk_bf16_f32 v5, v6, v7\n\t"
    "ds_write_b64 v245, v[4:5]\n\t"
    "v_mfma_f32_32x32x16_bf16 v[64:79], v[184:187], v[228:231], v[64:79]\n\t"
    "v_mul_f32_e32 v8, v249, v8\n\t"
    "v_mul_f32_e32 v9, v249, v9\n\t"
    "v_mul_f32_e32 v10, v249, v10\n\t"
    "v_mul_f32_e32 v11, v249, v11\n\t"
    "v_cvt_pk_bf16_f32 v8, v8, v9\n\t"
    "v_cvt_pk_bf16_f32 v9, v10, v11\n\t"
    "ds_write_b64 v235, v[8:9]\n\t"
    "v_mfma_f32_32x32x16_bf16 v[80:95], v[164:167], v[224:227], v[80:95]\n\t"
    "v_mul_f32_e32 v12, v249, v12\n\t"
    "v_mul_f32_e32 v13, v249, v13\n\t"
    "v_mul_f32_e32 v14, v249, v14\n\t"
    "v_mul_f32_e32 v15, v249, v15\n\t"
    "v_cvt_pk_bf16_f32 v12, v12, v13\n\t"
    "v_cvt_pk_bf16_f32 v13, v14, v15\n\t"
    "ds_write_b64 v236, v[12:13]\n\t"
    "v_mfma_f32_32x32x16_bf16 v[80:95], v[188:191], v[228:231], v[80:95]\n\t"
    "v_xor_b32_e32 v247, 64, v250\n\t"
    "v_xor_b32_e32 v248, 80, v250\n\t"
    "v_xor_b32_e32 v232, 96, v250\n\t"
    "v_xor_b32_e32 v233, 112, v250\n\t"
    ".Lisc%=:\n\t"
    "s_cmp_lt_i32 s40, s60\n\t"
    "s_cbranch_scc0 .Lisdc%=\n\t"
    "s_add_u32 m0, s43, s44\n\t"
    "s_xor_b32 s44, s44, s45\n\t"
    "global_load_lds_dwordx4 v237, s[36:37]" KV_POL "\n\t"
    "global_load_lds_dwordx4 v238, s[36:37] offset:1024" KV_POL "\n\t"
    "global_load_lds_dwordx4 v239, s[36:37] offset:2048" KV_POL "\n\t"
    "s_xor_b32 s45, s45, 0x8000\n\t"
    "s_add_u32 s36, s36, s38\n\t"
    "s_addc_u32 s37, s37, s39\n\t"
    "s_add_i32 s40, s40, 1\n\t"
    "s_branch .Lisc%=\n\t"
    ".Lisdc%=:\n\t"
    "s_add_i32 s46, s46, 1\n\t"
    "s_sub_i32 s58, s40, s46\n\t"
    "s_add_i32 s58, s58, 2\n\t"
    "s_sub_i32 s58, s58, s42\n\t"
    "s_cmp_eq_u32 s46, s34\n\t"
    "s_cselect_b32 s58, 0, s58\n\t"
    "v_mul_f32_e32 v16, v249, v16\n\t"
    "v_mul_f32_e32 v17, v249, v17\n\t"
    "v_mul_f32_e32 v18, v249, v18\n\t"
    "v_mul_f32_e32 v19, v249, v19\n\t"
    "v_cvt_pk_bf16_f32 v16, v16, v17\n\t"
    "v_cvt_pk_bf16_f32 v17, v18, v19\n\t"
    "ds_write_b64 v247, v[16:17]\n\t"
    "v_mul_f32_e32 v20, v249, v20\n\t"
    "v_mul_f32_e32 v21, v249, v21\n\t"
    "v_mul_f32_e32 v22, v249, v22\n\t"
    "v_mul_f32_e32 v23, v249, v23\n\t"
    "v_cvt_pk_bf16_f32 v20, v20, v21\n\t"
    "v_cvt_pk_bf16_f32 v21, v22, v23\n\t"
    "ds_write_b64 v248, v[20:21]\n\t"
    "v_mul_f32_e32 v24, v249, v24\n\t"
    "v_mul_f32_e32 v25, v249, v25\n\t"
    "v_mul_f32_e32 v26, v249, v26\n\t"
    "v_mul_f32_e32 v27, v249, v27\n\t"
    "v_cvt_pk_bf16_f32 v24, v24, v25\n\t"
    "v_cvt_pk_bf16_f32 v25, v26, v27\n\t"
    "ds_write_b64 v232, v[24:25]\n\t"
    "v_mul_f32_e32 v28, v249, v28\n\t"
    "v_mul_f32_e32 v29, v249, v29\n\t"
    "v_mul_f32_e32 v30, v249, v30\n\t"
    "v_mul_f32_e32 v31, v249, v31\n\t"
    "v_cvt_pk_bf16_f32 v28, v28, v29\n\t"
    "v_cvt_pk_bf16_f32 v29, v30, v31\n\t"
    "ds_write_b64 v233, v[28:29]\n\t"
    "v_add_u32_e32 v241, 0x800, v253\n\t"
    "ds_read_b128 v[96:99], v253 offset:0\n\t"
    "ds_read2_b64 v[100:103], v253 offset0:129 offset1:128\n\t"
    "ds_read_b128 v[104:107], v253 offset:2048\n\t"
    "ds_read2_b64 v[108:111], v241 offset0:129 offset1:128\n\t"
    "v_add_u32_e32 v192, 0xc00, v254\n\t"
    "v_add_u32_e32 v193, 0x1800, v254\n\t"
    "v_add_u32_e32 v194, 0x2400, v254\n\t"
    "s_cmp_lt_i32 s58, 0\n\t"
    "s_cbranch_scc1 .Lvled%=\n\t"
    "s_cmp_lt_i32 s58, 2\n\t"
    "s_cbranch_scc1 .Lvle1%=\n\t"
    "s_waitcnt vmcnt(6)\n\t"
    "s_branch .Lvled%=\n\t"
    ".Lvle1%=:\n\t"
    "s_waitcnt vmcnt(3)\n\t"
    "s_cmp_lt_i32 s58, 1\n\t"
    "s_cbranch_scc0 .Lvled%=\n\t"
    "s_waitcnt vmcnt(0)\n\t"
    ".Lvled%=:\n\t"
    "s_cmp_gt_i32 s46, s34\n\t"
    "s_cbranch_scc1 .Lnobar%=\n\t"
    "s_cmp_ge_i32 s46, 4\n\t"
    "s_addc_u32 s60, s46, s42\n\t"
    "s_min_i32 s60, s60, s41\n\t"
    "s_barrier\n\t"
    ".Lisd%=:\n\t"
    "s_cmp_lt_i32 s40, s60\n\t"
    "s_cbranch_scc0 .Lisdd%=\n\t"
    "s_add_u32 m0, s43, s44\n\t"
    "s_xor_b32 s44, s44, s45\n\t"
    "global_load_lds_dwordx4 v237, s[36:37]" KV_POL "\n\t"
    "global_load_lds_dwordx4 v238, s[36:37] offset:1024" KV_POL "\n\t"
    "global_load_lds_dwordx4 v239, s[36:37] offset:2048" KV_POL "\n\t"
    "s_xor_b32 s45, s45, 0x8000\n\t"
    "s_add_u32 s36, s36, s38\n\t"
    "s_addc_u32 s37, s37, s39\n\t"
    "s_add_i32 s40, s40, 1\n\t"
    "s_branch .Lisd%=\n\t"
    ".Lisdd%=:\n\t"
    "s_add_i32 s46, s46, 1\n\t"
    ".Lnobar%=:\n\t"
    "s_mov_b32 s67, s40\n\t"
    "v_mul_f32_e32 v32, v249, v32\n\t"
    "v_mul_f32_e32 v33, v249, v33\n\t"
    "v_mul_f32_e32 v34, v249, v34\n\t"
    "v_mul_f32_e32 v35, v249, v35\n\t"
    "v_cvt_pk_bf16_f32 v32, v32, v33\n\t"
    "v_cvt_pk_bf16_f32 v33, v34, v35\n\t"
    "ds_write_b64 v250, v[32:33]\n\t"
    "v_mul_f32_e32 v36, v249, v36\n\t"
    "v_mul_f32_e32 v37, v249, v37\n\t"
    "v_mul_f32_e32 v38, v249, v38\n\t"
    "v_mul_f32_e32 v39, v249, v39\n\t"
    "v_cvt_pk_bf16_f32 v36, v36, v37\n\t"
    "v_cvt_pk_bf16_f32 v37, v38, v39\n\t"
    "ds_write_b64 v245, v[36:37]\n\t"
    "s_waitcnt lgkmcnt(2)\n\t"
    "global_store_dwordx4 v254, v[96:99], s[74:75] offset:0" O_POL "\n\t"
    "v_mul_f32_e32 v40, v249, v40\n\t"
    "v_mul_f32_e32 v41, v249, v41\n\t"
    "v_mul_f32_e32 v42, v249, v42\n\t"
    "v_mul_f32_e32 v43, v249, v43\n\t"
    "v_cvt_pk_bf16_f32 v40, v40, v41\n\t"
    "v_cvt_pk_bf16_f32 v41, v42, v43\n\t"
    "ds_write_b64 v235, v[40:41]\n\t"
    "global_store_dwordx4 v192, v[100:103], s[74:75] offset:0" O_POL "\n\t"
    "v_mul_f32_e32 v44, v249, v44\n\t"
    "v_mul_f32_e32 v45, v249, v45\n\t"
    "v_mul_f32_e32 v46, v249, v46\n\t"
    "v_mul_f32_e32 v47, v249, v47\n\t"
    "v_cvt_pk_bf16_f32 v44, v44, v45\n\t"
    "v_cvt_pk_bf16_f32 v45, v46, v47\n\t"
    "ds_write_b64 v236, v[44:45]\n\t"
    "global_store_dwordx4 v193, v[104:107], s[74:75] offset:0" O_POL "\n\t"
    "v_mul_f32_e32 v48, v249, v48\n\t"
    "v_mul_f32_e32 v49, v249, v49\n\t"
    "v_mul_f32_e32 v50, v249, v50\n\t"
    "v_mul_f32_e32 v51, v249, v51\n\t"
    "v_cvt_pk_bf16_f32 v48, v48, v49\n\t"
    "v_cvt_pk_bf16_f32 v49, v50, v51\n\t"
    "ds_write_b64 v247, v[48:49]\n\t"
    "global_store_dwordx4 v194, v[108:111], s[74:75] offset:0" O_POL "\n\t"
    "v_mul_f32_e32 v52, v249, v52\n\t"
    "v_mul_f32_e32 v53, v249, v53\n\t"
    "v_mul_f32_e32 v54, v249, v54\n\t"
    "v_mul_f32_e32 v55, v249, v55\n\t"
    "v_cvt_pk_bf16_f32 v52, v52, v53\n\t"
    "v_cvt_pk_bf16_f32 v53, v54, v55\n\t"
    "ds_write_b64 v248, v[52:53]\n\t"
    "v_mul_f32_e32 v56, v249, v56\n\t"
    "v_mul_f32_e32 v57, v249, v57\n\t"
    "v_mul_f32_e32 v58, v249, v58\n\t"
    "v_mul_f32_e32 v59, v249, v59\n\t"
    "v_cvt_pk_bf16_f32 v56, v56, v57\n\t"
    "v_cvt_pk_bf16_f32 v57, v58, v59\n\t"
    "ds_write_b64 v232, v[56:57]\n\t"
    "v_mul_f32_e32 v60, v249, v60\n\t"
    "v_mul_f32_e32 v61, v249, v61\n\t"
    "v_mul_f32_e32 v62, v249, v62\n\t"
    "v_mul_f32_e32 v63, v249, v63\n\t"
    "v_cvt_pk_bf16_f32 v60, v60, v61\n\t"
    "v_cvt_pk_bf16_f32 v61, v62, v63\n\t"
    "ds_write_b64 v233, v[60:61]\n\t"
    "ds_read_b128 v[112:115], v253 offset:0\n\t"
    "ds_read2_b64 v[116:119], v253 offset0:129 offset1:128\n\t"
    "ds_read_b128 v[120:123], v253 offset:2048\n\t"
    "ds_read2_b64 v[124:127], v241 offset0:129 offset1:128\n\t"
    "v_mul_f32_e32 v64, v249, v64\n\t"
    "v_mul_f32_e32 v65, v249, v65\n\t"
    "v_mul_f32_e32 v66, v249, v66\n\t"
    "v_mul_f32_e32 v67, v249, v67\n\t"
    "v_cvt_pk_bf16_f32 v64, v64, v65\n\t"
    "v_cvt_pk_bf16_f32 v65, v66, v67\n\t"
    "ds_write_b64 v250, v[64:65]\n\t"
    "v_mul_f32_e32 v68, v249, v68\n\t"
    "v_mul_f32_e32 v69, v249, v69\n\t"
    "v_mul_f32_e32 v70, v249, v70\n\t"
    "v_mul_f32_e32 v71, v249, v71\n\t"
    "v_cvt_pk_bf16_f32 v68, v68, v69\n\t"
    "v_cvt_pk_bf16_f32 v69, v70, v71\n\t"
    "ds_write_b64 v245, v[68:69]\n\t"
    "s_waitcnt lgkmcnt(2)\n\t"
    "global_store_dwordx4 v254, v[112:115], s[74:75] offset:128" O_POL "\n\t"
    "v_mul_f32_e32 v72, v249, v72\n\t"
    "v_mul_f32_e32 v73, v249, v73\n\t"
    "v_mul_f32_e32 v74, v249, v74\n\t"
    "v_mul_f32_e32 v75, v249, v75\n\t"
    "v_cvt_pk_bf16_f32 v72, v72, v73\n\t"
    "v_cvt_pk_bf16_f32 v73, v74, v75\n\t"
    "ds_write_b64 v235, v[72:73]\n\t"
    "global_store_dwordx4 v192, v[116:119], s[74:75] offset:128" O_POL "\n\t"
    "v_mul_f32_e32 v76, v249, v76\n\t"
    "v_mul_f32_e32 v77, v249, v77\n\t"
    "v_mul_f32_e32 v78, v249, v78\n\t"
    "v_mul_f32_e32 v79, v249, v79\n\t"
    "v_cvt_pk_bf16_f32 v76, v76, v77\n\t"
    "v_cvt_pk_bf16_f32 v77, v78, v79\n\t"
    "ds_write_b64 v236, v[76:77]\n\t"
    "global_store_dwordx4 v193, v[120:123], s[74:75] offset:128" O_POL "\n\t"
    "v_mul_f32_e32 v80, v249, v80\n\t"
    "v_mul_f32_e32 v81, v249, v81\n\t"
    "v_mul_f32_e32 v82, v249, v82\n\t"
    "v_mul_f32_e32 v83, v249, v83\n\t"
    "v_cvt_pk_bf16_f32 v80, v80, v81\n\t"
    "v_cvt_pk_bf16_f32 v81, v82, v83\n\t"
    "ds_write_b64 v247, v[80:81]\n\t"
    "global_store_dwordx4 v194, v[124:127], s[74:75] offset:128" O_POL "\n\t"
    "v_mul_f32_e32 v84, v249, v84\n\t"
    "v_mul_f32_e32 v85, v249, v85\n\t"
    "v_mul_f32_e32 v86, v249, v86\n\t"
    "v_mul_f32_e32 v87, v249, v87\n\t"
    "v_cvt_pk_bf16_f32 v84, v84, v85\n\t"
    "v_cvt_pk_bf16_f32 v85, v86, v87\n\t"
    "ds_write_b64 v248, v[84:85]\n\t"
    "v_mul_f32_e32 v88, v249, v88\n\t"
    "v_mul_f32_e32 v89, v249, v89\n\t"
    "v_mul_f32_e32 v90, v249, v90\n\t"
    "v_mul_f32_e32 v91, v249, v91\n\t"
    "v_cvt_pk_bf16_f32 v88, v88, v89\n\t"
    "v_cvt_pk_bf16_f32 v89, v90, v91\n\t"
    "ds_write_b64 v232, v[88:89]\n\t"
    "v_mul_f32_e32 v92, v249, v92\n\t"
    "v_mul_f32_e32 v93, v249, v93\n\t"
    "v_mul_f32_e32 v94, v249, v94\n\t"
    "v_mul_f32_e32 v95, v249, v95\n\t"
    "v_cvt_pk_bf16_f32 v92, v92, v93\n\t"
    "v_cvt_pk_bf16_f32 v93, v94, v95\n\t"
    "ds_write_b64 v233, v[92:93]\n\t"
    "ds_read_b128 v[128:131], v253 offset:0\n\t"
    "ds_read2_b64 v[132:135], v253 offset0:129 offset1:128\n\t"
    "ds_read_b128 v[136:139], v253 offset:2048\n\t"
    "ds_read2_b64 v[140:143], v241 offset0:129 offset1:128\n\t"
    "s_waitcnt lgkmcnt(3)\n\t"
    "global_store_dwordx4 v254, v[128:131], s[74:75] offset:256" O_POL "\n\t"
    "s_waitcnt lgkmcnt(2)\n\t"
    "global_store_dwordx4 v192, v[132:135], s[74:75] offset:256" O_POL "\n\t"
    "s_waitcnt lgkmcnt(1)\n\t"
    "global_store_dwordx4 v193, v[136:139], s[74:75] offset:256" O_POL "\n\t"
    "s_waitcnt lgkmcnt(0)\n\t"
    "global_store_dwordx4 v194, v[140:143], s[74:75] offset:256" O_POL "\n\t"
    "s_mov_b32 s68, s40\n\t"
    "s_mov_b32 s86, 0\n\t"
    "s_mov_b32 s79, s41\n\t"
    "s_call_b64 s[76:77], .Lidle%=\n\t"
    "s_branch .Lend%=\n\t"
    ".Lidle%=:\n\t"
    "s_cmp_lt_i32 s46, s79\n\t"
    "s_cbranch_scc0 .Lidled%=\n\t"
    "s_cmp_gt_i32 s46, s34\n\t"
    "s_cbranch_scc1 .Lidlex%=\n\t"
    "s_add_i32 s59, s46, s42\n\t"
    "s_sub_i32 s59, s59, 3\n\t"
    "s_sub_i32 s58, s41, 1\n\t"
    "s_cmp_eq_u32 s46, s34\n\t"
    "s_cselect_b32 s59, s58, s59\n\t"
    "s_cmp_lt_i32 s59, s68\n\t"
    "s_cselect_b32 s58, 2, 0\n\t"
    "s_cmp_lt_i32 s59, s67\n\t"
    "s_cselect_b32 s58, 4, s58\n\t"
    "s_add_i32 s58, s58, s40\n\t"
    "s_sub_i32 s58, s58, s59\n\t"
    "s_add_i32 s58, s58, s86\n\t"
    "s_min_i32 s58, s58, 8\n\t"
    "s_cmp_lt_i32 s59, s41\n\t"
    "s_cselect_b32 s58, s58, 9\n\t"
    "s_lshl_b32 s58, s58, 3\n\t"
    "s_add_i32 s58, s58, 4\n\t"
    "s_cmp_ge_i32 s46, 4\n\t"
    "s_addc_u32 s60, s46, s42\n\t"
    "s_min_i32 s60, s60, s41\n\t"
    "s_getpc_b64 vcc\n\t"
    "s_add_u32 vcc_lo, vcc_lo, s58\n\t"
    "s_addc_u32 vcc_hi, vcc_hi, 0\n\t"
    "s_setpc_b64 vcc\n\t"
    "s_waitcnt vmcnt(0)\n\t"
    "s_branch .Lidlew%=\n\t"
    "s_waitcnt vmcnt(3)\n\t"
    "s_branch .Lidlew%=\n\t"
    "s_waitcnt vmcnt(6)\n\t"
    "s_branch .Lidlew%=\n\t"
    "s_waitcnt vmcnt(9)\n\t"
    "s_branch .Lidlew%=\n\t"
    "s_waitcnt vmcnt(12)\n\t"
    "s_branch .Lidlew%=\n\t"
    "s_waitcnt vmcnt(15)\n\t"
    "s_branch .Lidlew%=\n\t"
    "s_waitcnt vmcnt(18)\n\t"
    "s_branch .Lidlew%=\n\t"
    "s_waitcnt vmcnt(21)\n\t"
    "s_branch .Lidlew%=\n\t"
    ".Lidlew%=:\n\t"
    "s_barrier\n\t"
    ".Lisi%=:\n\t"
    "s_cmp_lt_i32 s40, s60\n\t"
    "s_cbranch_scc0 .Lisdi%=\n\t"
    "s_add_u32 m0, s43, s44\n\t"
    "s_xor_b32 s44, s44, s45\n\t"
    "global_load_lds_dwordx4 v237, s[36:37]" KV_POL "\n\t"
    "global_load_lds_dwordx4 v238, s[36:37] offset:1024" KV_POL "\n\t"
    "global_load_lds_dwordx4 v239, s[36:37] offset:2048" KV_POL "\n\t"
    "s_xor_b32 s45, s45, 0x8000\n\t"
    "s_add_u32 s36, s36, s38\n\t"
    "s_addc_u32 s37, s37, s39\n\t"
    "s_add_i32 s40, s40, 1\n\t"
    "s_branch .Lisi%=\n\t"
    ".Lisdi%=:\n\t"
    "s_add_i32 s46, s46, 1\n\t"
    "s_branch .Lidle%=\n\t"
    ".Lidlex%=:\n\t"
    "s_mov_b32 s46, s79\n\t"
    ".Lidled%=:\n\t"
    "s_setpc_b64 s[76:77]\n\t"
    ".Lslow%=:\n\t"
    "s_cmp_gt_i32 s46, s34\n\t"
    "s_cbranch_scc1 .Lfree%=\n\t"
    "s_sub_i32 s58, s40, s46\n\t"
    "s_add_i32 s58, s58, 2\n\t"
    "s_sub_i32 s58, s58, s42\n\t"
    "s_cmp_eq_u32 s46, s34\n\t"
    "s_cselect_b32 s58, 0, s58\n\t"
    "s_cmp_lt_i32 s58, 0\n\t"
    "s_cbranch_scc1 .Lvlsd%=\n\t"
    "s_cmp_lt_i32 s58, 2\n\t"
    "s_cbranch_scc1 .Lvls1%=\n\t"
    "s_waitcnt vmcnt(6)\n\t"
    "s_branch .Lvlsd%=\n\t"
    ".Lvls1%=:\n\t"
    "s_waitcnt vmcnt(3)\n\t"
    "s_cmp_lt_i32 s58, 1\n\t"
    "s_cbranch_scc0 .Lvlsd%=\n\t"
    "s_waitcnt vmcnt(0)\n\t"
    ".Lvlsd%=:\n\t"
    "s_waitcnt lgkmcnt(0)\n\t"
    "s_branch .Lslowd%=\n\t"
    ".Lfree%=:\n\t"
    "s_waitcnt lgkmcnt(0)\n\t"
    "s_cmp_lt_i32 s46, s47\n\t"
    "s_cbranch_scc1 .Lloopnb%=\n\t"
    "s_branch .Llast%=\n\t"
    ".Lmask1%=:\n\t"
    "s_add_i32 s58, s46, 1\n\t"
    "v_mov_b32_e32 v247, s63\n\t"
    "s_cmp_eq_u32 s58, s47\n\t"
    "s_cbranch_scc0 .Lmask2%=\n\t"
    "v_cndmask_b32_e64 v192, v247, v192, s[4:5]\n\t"
    "v_cndmask_b32_e64 v193, v247, v193, s[6:7]\n\t"
    "v_cndmask_b32_e64 v194, v247, v194, s[8:9]\n\t"
    "v_cndmask_b32_e64 v195, v247, v195, s[10:11]\n\t"
    "v_cndmask_b32_e64 v196, v247, v196, s[12:13]\n\t"
    "v_cndmask_b32_e64 v197, v247, v197, s[14:15]\n\t"
    "v_cndmask_b32_e64 v198, v247, v198, s[16:17]\n\t"
    "v_cndmask_b32_e64 v199, v247, v199, s[18:19]\n\t"
    "v_cndmask_b32_e64 v200, v247, v200, s[20:21]\n\t"
    "v_cndmask_b32_e64 v201, v247, v201, s[22:23]\n\t"
    "v_cndmask_b32_e64 v202, v247, v202, s[24:25]\n\t"
    "v_cndmask_b32_e64 v203, v247, v203, s[26:27]\n\t"
    "v_cndmask_b32_e64 v204, v247, v204, s[28:29]\n\t"
    "v_cndmask_b32_e64 v205, v247, v205, s[30:31]\n\t"
    "v_cndmask_b32_e64 v206, v247, v206, s[94:95]\n\t"
    "v_cndmask_b32_e64 v207, v247, v207, s[96:97]\n\t"
    "s_branch .Lmaskd1%=\n\t"
    ".Lmask2%=:\n\t"
    "s_call_b64 s[76:77], .Lmask%=\n\t"
    "s_branch .Lmaskd1%=\n\t"
    ".Lmask%=:\n\t"
    "s_mul_i32 s58, s46, s54\n\t"
    "s_add_i32 s58, s58, s53\n\t"
    "v_subrev_u32_e32 v247, s58, v246\n\t"
    "v_subrev_u32_e32 v248, s55, v247\n\t"
    "v_med3_i32 v247, v247, 0, 31\n\t"
    "v_med3_i32 v248, v248, 0, 31\n\t"
    "v_bfm_b32 v247, v247, 0\n\t"
    "v_bfm_b32 v248, v248, 0\n\t"
    "v_bfi_b32 v247, v248, 0, v247\n\t"
    "v_bfe_i32 v248, v247, 0, 1\n\t"
    "v_bfi_b32 v192, v248, v192, s63\n\t"
    "v_bfe_i32 v249, v247, 1, 1\n\t"
    "v_bfi_b32 v193, v249, v193, s63\n\t"
    "v_bfe_i32 v248, v247, 2, 1\n\t"
    "v_bfi_b32 v194, v248, v194, s63\n\t"
    "v_bfe_i32 v249, v247, 3, 1\n\t"
    "v_bfi_b32 v195, v249, v195, s63\n\t"
    "v_bfe_i32 v248, v247, 8, 1\n\t"
    "v_bfi_b32 v196, v248, v196, s63\n\t"
    "v_bfe_i32 v249, v247, 9, 1\n\t"
    "v_bfi_b32 v197, v249, v197, s63\n\t"
    "v_bfe_i32 v248, v247, 10, 1\n\t"
    "v_bfi_b32 v198, v248, v198, s63\n\t"
    "v_bfe_i32 v249, v247, 11, 1\n\t"
    "v_bfi_b32 v199, v249, v199, s63\n\t"
    "v_bfe_i32 v248, v247, 16, 1\n\t"
    "v_bfi_b32 v200, v248, v200, s63\n\t"
    "v_bfe_i32 v249, v247, 17, 1\n\t"
    "v_bfi_b32 v201, v249, v201, s63\n\t"
    "v_bfe_i32 v248, v247, 18, 1\n\t"
    "v_bfi_b32 v202, v248, v202, s63\n\t"
    "v_bfe_i32 v249, v247, 19, 1\n\t"
    "v_bfi_b32 v203, v249, v203, s63\n\t"
    "v_bfe_i32 v248, v247, 24, 1\n\t"
    "v_bfi_b32 v204, v248, v204, s63\n\t"
    "v_bfe_i32 v249, v247, 25, 1\n\t"
    "v_bfi_b32 v205, v249, v205, s63\n\t"
    "v_bfe_i32 v248, v247, 26, 1\n\t"
    "v_bfi_b32 v206, v248, v206, s63\n\t"
    "v_bfe_i32 v249, v247, 27, 1\n\t"
    "v_bfi_b32 v207, v249, v207, s63\n\t"
    "s_setpc_b64 s[76:77]\n\t"
    ".Lrare%=:\n\t"
    "v_mov_b32_e32 v247, v244\n\t"
    "v_cmp_eq_f32_e64 s[64:65], s63, v241\n\t"
    "s_nop 0\n\t"
    "v_permlane32_swap_b32_e32 v244, v247\n\t"
    "v_max_f32_e32 v244, v244, v247\n\t"
    "v_mul_f32_e32 v247, s52, v244\n\t"
    "v_sub_f32_e32 v248, v247, v240\n\t"
    "v_cndmask_b32_e64 v249, v248, |v248|, s[64:65]\n\t"
    "v_cmp_lt_f32_e32 vcc, 0x41000000, v249\n\t"
    "v_cmp_neq_f32_e64 s[76:77], s63, v247\n\t"
    "s_nop 0\n\t"
    "s_and_b64 vcc, vcc, s[76:77]\n\t"
    "s_orn2_b64 s[76:77], s[76:77], s[64:65]\n\t"
    "v_cndmask_b32_e32 v247, v240, v247, vcc\n\t"
    "v_add_f32_e32 v249, 0x41000000, v247\n\t"
    "v_mul_f32_e32 v249, s62, v249\n\t"
    "v_cndmask_b32_e64 v241, v241, v249, s[76:77]\n\t"
    "s_cbranch_vccz .Lrared%=\n\t"
    "v_sub_f32_e32 v248, v240, v247\n\t"
    "v_exp_f32_e32 v248, v248\n\t"
    "v_mov_b32_e32 v240, v247\n\t"
    "s_andn2_b64 vcc, vcc, s[64:65]\n\t"
    "s_cbranch_scc0 .Lrnoo%=\n\t"
    "v_cndmask_b32_e32 v248, 1.0, v248, vcc\n\t"
    "s_nop 7\n\t"
    "v_mul_f32_e32 v242, v248, v242\n\t"
    "v_mul_f32_e32 v243, v248, v243\n\t"
    "v_mul_f32_e32 v0, v248, v0\n\t"
    "v_mul_f32_e32 v1, v248, v1\n\t"
    "v_mul_f32_e32 v2, v248, v2\n\t"
    "v_mul_f32_e32 v3, v248, v3\n\t"
    "v_mul_f32_e32 v4, v248, v4\n\t"
    "v_mul_f32_e32 v5, v248, v5\n\t"
    "v_mul_f32_e32 v6, v248, v6\n\t"
    "v_mul_f32_e32 v7, v248, v7\n\t"
    "v_mul_f32_e32 v8, v248, v8\n\t"
    "v_mul_f32_e32 v9, v248, v9\n\t"
    "v_mul_f32_e32 v10, v248, v10\n\t"
    "v_mul_f32_e32 v11, v248, v11\n\t"
    "v_mul_f32_e32 v12, v248, v12\n\t"
    "v_mul_f32_e32 v13, v248, v13\n\t"
    "v_mul_f32_e32 v14, v248, v14\n\t"
    "v_mul_f32_e32 v15, v248, v15\n\t"
    "v_mul_f32_e32 v16, v248, v16\n\t"
    "v_mul_f32_e32 v17, v248, v17\n\t"
    "v_mul_f32_e32 v18, v248, v18\n\t"
    "v_mul_f32_e32 v19, v248, v19\n\t"
    "v_mul_f32_e32 v20, v248, v20\n\t"
    "v_mul_f32_e32 v21, v248, v21\n\t"
    "v_mul_f32_e32 v22, v248, v22\n\t"
    "v_mul_f32_e32 v23, v248, v23\n\t"
    "v_mul_f32_e32 v24, v248, v24\n\t"
    "v_mul_f32_e32 v25, v248, v25\n\t"
    "v_mul_f32_e32 v26, v248, v26\n\t"
    "v_mul_f32_e32 v27, v248, v27\n\t"
    "v_mul_f32_e32 v28, v248, v28\n\t"
    "v_mul_f32_e32 v29, v248, v29\n\t"
    "v_mul_f32_e32 v30, v248, v30\n\t"
    "v_mul_f32_e32 v31, v248, v31\n\t"
    "v_mul_f32_e32 v32, v248, v32\n\t"
    "v_mul_f32_e32 v33, v248, v33\n\t"
    "v_mul_f32_e32 v34, v248, v34\n\t"
    "v_mul_f32_e32 v35, v248, v35\n\t"
    "v_mul_f32_e32 v36, v248, v36\n\t"
    "v_mul_f32_e32 v37, v248, v37\n\t"
    "v_mul_f32_e32 v38, v248, v38\n\t"
    "v_mul_f32_e32 v39, v248, v39\n\t"
    "v_mul_f32_e32 v40, v248, v40\n\t"
    "v_mul_f32_e32 v41, v248, v41\n\t"
    "v_mul_f32_e32 v42, v248, v42\n\t"
    "v_mul_f32_e32 v43, v248, v43\n\t"
    "v_mul_f32_e32 v44, v248, v44\n\t"
    "v_mul_f32_e32 v45, v248, v45\n\t"
    "v_mul_f32_e32 v46, v248, v46\n\t"
    "v_mul_f32_e32 v47, v248, v47\n\t"
    "v_mul_f32_e32 v48, v248, v48\n\t"
    "v_mul_f32_e32 v49, v248, v49\n\t"
    "v_mul_f32_e32 v50, v248, v50\n\t"
    "v_mul_f32_e32 v51, v248, v51\n\t"
    "v_mul_f32_e32 v52, v248, v52\n\t"
    "v_mul_f32_e32 v53, v248, v53\n\t"
    "v_mul_f32_e32 v54, v248, v54\n\t"
    "v_mul_f32_e32 v55, v248, v55\n\t"
    "v_mul_f32_e32 v56, v248, v56\n\t"
    "v_mul_f32_e32 v57, v248, v57\n\t"
    "v_mul_f32_e32 v58, v248, v58\n\t"
    "v_mul_f32_e32 v59, v248, v59\n\t"
    "v_mul_f32_e32 v60, v248, v60\n\t"
    "v_mul_f32_e32 v61, v248, v61\n\t"
    "v_mul_f32_e32 v62, v248, v62\n\t"
    "v_mul_f32_e32 v63, v248, v63\n\t"
    "v_mul_f32_e32 v64, v248, v64\n\t"
    "v_mul_f32_e32 v65, v248, v65\n\t"
    "v_mul_f32_e32 v66, v248, v66\n\t"
    "v_mul_f32_e32 v67, v248, v67\n\t"
    "v_mul_f32_e32 v68, v248, v68\n\t"
    "v_mul_f32_e32 v69, v248, v69\n\t"
    "v_mul_f32_e32 v70, v248, v70\n\t"
    "v_mul_f32_e32 v71, v248, v71\n\t"
    "v_mul_f32_e32 v72, v248, v72\n\t"
    "v_mul_f32_e32 v73, v248, v73\n\t"
    "v_mul_f32_e32 v74, v248, v74\n\t"
    "v_mul_f32_e32 v75, v248, v75\n\t"
    "v_mul_f32_e32 v76, v248, v76\n\t"
    "v_mul_f32_e32 v77, v248, v77\n\t"
    "v_mul_f32_e32 v78, v248, v78\n\t"
    "v_mul_f32_e32 v79, v248, v79\n\t"
    "v_mul_f32_e32 v80, v248, v80\n\t"
    "v_mul_f32_e32 v81, v248, v81\n\t"
    "v_mul_f32_e32 v82, v248, v82\n\t"
    "v_mul_f32_e32 v83, v248, v83\n\t"
    "v_mul_f32_e32 v84, v248, v84\n\t"
    "v_mul_f32_e32 v85, v248, v85\n\t"
    "v_mul_f32_e32 v86, v248, v86\n\t"
    "v_mul_f32_e32 v87, v248, v87\n\t"
    "v_mul_f32_e32 v88, v248, v88\n\t"
    "v_mul_f32_e32 v89, v248, v89\n\t"
    "v_mul_f32_e32 v90, v248, v90\n\t"
    "v_mul_f32_e32 v91, v248, v91\n\t"
    "v_mul_f32_e32 v92, v248, v92\n\t"
    "v_mul_f32_e32 v93, v248, v93\n\t"
    "v_mul_f32_e32 v94, v248, v94\n\t"
    "v_mul_f32_e32 v95, v248, v95\n\t"
    ".Lrnoo%=:\n\t"
    "v_fma_f32 v208, v192, s52, -v240\n\t"
    "v_fma_f32 v209, v193, s52, -v240\n\t"
    "v_fma_f32 v210, v194, s52, -v240\n\t"
    "v_fma_f32 v211, v195, s52, -v240\n\t"
    "v_fma_f32 v212, v196, s52, -v240\n\t"
    "v_fma_f32 v213, v197, s52, -v240\n\t"
    "v_fma_f32 v214, v198, s52, -v240\n\t"
    "v_fma_f32 v215, v199, s52, -v240\n\t"
    "v_fma_f32 v216, v200, s52, -v240\n\t"
    "v_fma_f32 v217, v201, s52, -v240\n\t"
    "v_fma_f32 v218, v202, s52, -v240\n\t"
    "v_fma_f32 v219, v203, s52, -v240\n\t"
    "v_fma_f32 v220, v204, s52, -v240\n\t"
    "v_fma_f32 v221, v205, s52, -v240\n\t"
    "v_fma_f32 v222, v206, s52, -v240\n\t"
    "v_fma_f32 v223, v207, s52, -v240\n\t"
    "v_exp_f32_e32 v208, v208\n\t"
    "v_exp_f32_e32 v209, v209\n\t"
    "v_exp_f32_e32 v210, v210\n\t"
    "v_exp_f32_e32 v211, v211\n\t"
    "s_branch .Lrared%=\n\t"
    ".Lrespec%=:\n\t"
    "s_waitcnt vmcnt(0)\n\t"
    "s_barrier\n\t"
    "s_mov_b32 s66, s61\n\t"
    "s_cmp_gt_i32 s66, 0\n\t"
    "s_cbranch_scc1 .Lsetup%=\n\t"
    ".Lend%=:\n\t"
        : "=&{s66}"(W), "+{v255}"(tid)
        : "s"(__builtin_amdgcn_kernarg_segment_ptr()), "s"(blockIdx.x), "s"((unsigned)(uintptr_t)lds_raw)
        : "memory", "scc", "vcc", "s33", "s34", "s35", "s92", "s93", "s94", "s95", "s96", "s97", "s98", "s99",
          "s4", "s5", "s6", "s7", "s8", "s9", "s10", "s11", "s12", "s13", "s14", "s15", "s16", "s17", "s18", "s19", "s20", "s21", "s22", "s23", "s24", "s25", "s26", "s27", "s28", "s29", "s30", "s31", "s36", "s37", "s38", "s39", "s40", "s41", "s42", "s43", "s44", "s45", "s46", "s47", "s48", "s49", "s50", "s51", "s52", "s53", "s54", "s55", "s56", "s57", "s58", "s59", "s60", "s61", "s62", "s63", "s64", "s65", "s67", "s68", "s69", "s70", "s71", "s72", "s73", "s74", "s75", "s76", "s77", "s78", "s79", "s80", "s81", "s82", "s83", "s84", "s85", "s86", "s87", "s88", "s89", "s90", "s91",
          "v0", "v1", "v2", "v3", "v4", "v5", "v6", "v7", "v8", "v9", "v10", "v11", "v12", "v13", "v14", "v15", "v16", "v17", "v18", "v19", "v20", "v21", "v22", "v23", "v24", "v25", "v26", "v27", "v28", "v29", "v30", "v31", "v32", "v33", "v34", "v35", "v36", "v37", "v38", "v39", "v40", "v41", "v42", "v43", "v44", "v45", "v46", "v47", "v48", "v49", "v50", "v51", "v52", "v53", "v54", "v55", "v56", "v57", "v58", "v59", "v60", "v61", "v62", "v63", "v64", "v65", "v66", "v67", "v68", "v69", "v70", "v71", "v72", "v73", "v74", "v75", "v76", "v77", "v78", "v79", "v80", "v81", "v82", "v83", "v84", "v85", "v86", "v87", "v88", "v89", "v90", "v91", "v92", "v93", "v94", "v95", "v96", "v97", "v98", "v99", "v100", "v101", "v102", "v103", "v104", "v105", "v106", "v107", "v108", "v109", "v110", "v111", "v112", "v113", "v114", "v115", "v116", "v117", "v118", "v119", "v120", "v121", "v122", "v123", "v124", "v125", "v126", "v127", "v128", "v129", "v130", "v131", "v132", "v133", "v134", "v135", "v136", "v137", "v138", "v139", "v140", "v141", "v142", "v143", "v144", "v145", "v146", "v147", "v148", "v149", "v150", "v151", "v152", "v153", "v154", "v155", "v156", "v157", "v158", "v159", "v160", "v161", "v162", "v163", "v164", "v165", "v166", "v167", "v168", "v169", "v170", "v171", "v172", "v173", "v174", "v175", "v176", "v177", "v178", "v179", "v180", "v181", "v182", "v183", "v184", "v185", "v186", "v187", "v188", "v189", "v190", "v191", "v192", "v193", "v194", "v195", "v196", "v197", "v198", "v199", "v200", "v201", "v202", "v203", "v204", "v205", "v206", "v207", "v208", "v209", "v210", "v211", "v212", "v213", "v214", "v215", "v216", "v217", "v218", "v219", "v220", "v221", "v222", "v223", "v224", "v225", "v226", "v227", "v228", "v229", "v230", "v231", "v232", "v233", "v234", "v235", "v236", "v237", "v238", "v239", "v240", "v241", "v242", "v243", "v244", "v245", "v246", "v247", "v248", "v249", "v250", "v251", "v252", "v253", "v254");
    if (W <= 0) {
        const int bid = blockIdx.x;
        const size_t hoff = (size_t)((bid & 7) * heads_per_xcd + (bid >> 6)) * SEQ * ROWB;
        const __hip_bfloat16* Vh = (const __hip_bfloat16*)(V + hoff);
        if (tid < DH) {
            float acc = 0.f;
#pragma unroll 1
            for (int k = 0; k < SEQ; ++k) acc += __bfloat162float(Vh[(size_t)k * DH + tid]);
            const __hip_bfloat16 r = __float2bfloat16(acc * (1.0f / SEQ));
            __hip_bfloat16* Ob = (__hip_bfloat16*)(O + hoff + (size_t)((bid >> 3) & 7) * QB * ROWB);
#pragma unroll 1
            for (int q = 0; q < QB; ++q) Ob[(size_t)q * DH + tid] = r;
        }
    }
}

extern "C" void kernel_launch(void* const* d_in, const int* in_sizes, int n_in,
                              void* d_out, int out_size, void* d_ws, size_t ws_size,
                              hipStream_t stream) {
    (void)n_in; (void)d_ws; (void)ws_size; (void)out_size;
    const int heads = in_sizes[0] / (SEQ * DH);
    swa_fwd_kernel<<<dim3(heads * (SEQ / QB)), dim3(NW * 64), 0, stream>>>(
        (const char*)d_in[0], (const char*)d_in[1], (const char*)d_in[2], (const int*)d_in[3], (char*)d_out, heads / 8
        );
}
```
